```python
import torch

def _softmax(x, axis=-1):
    return torch.softmax(x, dim=axis)
def _gelu(x):
    return torch.nn.functional.gelu(x, approximate="tanh")
def _silu(x):
    return torch.nn.functional.silu(x)
def _sigmoid(x):
    return torch.sigmoid(x)
def _layernorm(x, axis=-1, eps=1e-5):
    m = x.mean(dim=axis, keepdim=True)
    v = x.var(dim=axis, keepdim=True, unbiased=False)
    return (x - m) / torch.sqrt(v + eps)
def _scatter_add(tgt, idx, src, axis=0):
    return tgt.clone().index_add_(axis, idx, src)
def _segment_sum(src, seg, n):
    out = torch.zeros((n,) + src.shape[1:], dtype=src.dtype)
    return out.index_add_(0, seg, src)
def _pad(x, pw):
    flat = tuple(v for pair in reversed(pw) for v in pair)
    return torch.nn.functional.pad(x, flat)

kernel_name = 'recurrent_scan__ssm_conv_h3__dense_activation_squared_relu__prompt'

def setup_inputs():

    _w = (lambda a: (lambda t: t.to(torch.bfloat16) if t.is_floating_point() else t)(torch.as_tensor(a)))
    def _A_setup():
        B, S, D = 1, 2048, 448
        ks = 4
        return {
            "q": _w((lambda *s: torch.randn(*s, dtype=torch.bfloat16) * 0.05)(B, S, D)),
            "k": _w((lambda *s: torch.randn(*s, dtype=torch.bfloat16) * 0.2)(B, S, D)),
            "v": _w((lambda *s: torch.randn(*s, dtype=torch.bfloat16) * 0.2)(B, S, D)),
            "shift_k": _w((lambda *s: torch.randn(*s, dtype=torch.bfloat16) * 0.2)(D, ks) * 0.5),
            "ssm_k": _w((lambda *s: torch.randn(*s, dtype=torch.bfloat16) * 0.2)(D, S) * (1.0 / S ** 0.5)),
            "ssm_D": _w((lambda *s: torch.randn(*s, dtype=torch.bfloat16) * 0.2)(D)),
        }
    _A_in = _A_setup()

    def _B_setup():
        B, S, D, F = 1, 2048, 448, 1792
        return {
            "x": _w((lambda *s: torch.randn(*s, dtype=torch.bfloat16) * 0.05)(B, S, D)),
            "w1": _w((lambda *s: torch.randn(*s, dtype=torch.bfloat16) * 0.1)(D, F)),
            "b1": _w((lambda *s: torch.randn(*s, dtype=torch.bfloat16) * 0.4)(F)),
            "w2": _w((lambda *s: torch.randn(*s, dtype=torch.bfloat16) * 0.1)(F, D)),
            "b2": _w((lambda *s: torch.randn(*s, dtype=torch.bfloat16) * 12.8)(D)),
        }
    _B_in = _B_setup()

    return {
        "A_q": _A_in["q"],
        "A_k": _A_in["k"],
        "A_v": _A_in["v"],
        "A_shift_k": _A_in["shift_k"],
        "A_ssm_k": _A_in["ssm_k"],
        "A_ssm_D": _A_in["ssm_D"],
        "B_x": _B_in["x"],
        "B_w1": _B_in["w1"],
        "B_b1": _B_in["b1"],
        "B_w2": _B_in["w2"],
        "B_b2": _B_in["b2"],
    }

def reference(A_q, A_k, A_v, A_shift_k, A_ssm_k, A_ssm_D, B_x, B_w1, B_b1, B_w2, B_b2):
    def _A_ref(q, k, v, shift_k, ssm_k, ssm_D):
        B, S, D = q.shape
        ks = shift_k.shape[-1]
        k_pad = _pad(k, ((0, 0), (ks - 1, 0), (0, 0)))
        k_shift = torch.zeros_like(k)
        for t in range(ks):
            k_shift = k_shift + k_pad[:, t:t + S, :] * shift_k[:, ks - 1 - t][None, None, :]
        kv = k_shift * v
        L = 2 * S
        kv_pad = _pad(kv, ((0, 0), (0, S), (0, 0)))
        kf_pad = _pad(ssm_k, ((0, 0), (0, S)))
        kv_f = (lambda x, axes=(-2,-1): torch.fft.rfft2(x.float(), dim=axes))(kv_pad[:, None, :, :], axes=(1, 2))
        kf_f = (lambda x, axes=(-2,-1): torch.fft.rfft2(x.float(), dim=axes))(kf_pad[:, None, :], axes=(1, 2))
        kf_f = (lambda x, *d: x.permute(*d))(kf_f, 1, 2, 0)
        y_f = kv_f * kf_f[None, :, :, :]
        y = (lambda x, s, axes=(-2,-1): torch.fft.irfft2(x, s=s, dim=axes).to(torch.bfloat16))(y_f, (1, L), axes=(1, 2))[:, 0, :S, :]
        y = y + kv * ssm_D[None, None, :]
        return q * y

    def _B_ref(x, w1, b1, w2, b2):
        h = torch.einsum("bsd,df->bsf", x, w1) + b1
        zero = torch.zeros_like(h)
        r = torch.where(h > zero, h, zero)
        h = r * r
        out = torch.einsum("bsf,fd->bsd", h, w2) + b2
        return out

    x = A_q
    xn = _layernorm(x, axis=-1)
    s = _A_ref(xn, A_k, A_v, A_shift_k, A_ssm_k, A_ssm_D)
    g = _silu(_B_ref(xn, B_w1, B_b1, B_w2, B_b2))
    return x + s * g
```

```cpp
#include <hip/hip_runtime.h>
#include <hip/hip_bf16.h>
#include <stdint.h>

typedef unsigned short u16;
#define S_ 2048
#define D_ 448
#define F_ 1792

__device__ __forceinline__ float bf2f(u16 v) { return __uint_as_float(((unsigned)v) << 16); }
__device__ __forceinline__ u16 f2bf(float f) {
  unsigned u = __float_as_uint(f);
  u += 0x7fffu + ((u >> 16) & 1u);
  return (u16)(u >> 16);
}
__device__ __forceinline__ float rbf(float f) { return bf2f(f2bf(f)); }

__global__ void n_ln(const u16* x, u16* xn) {
  int row = blockIdx.x;
  __shared__ float red[64];
  __shared__ float sm, sv;
  const u16* xr = x + row * D_;
  int t = threadIdx.x;
  float s = 0;
  for (int i = t; i < D_; i += 64) s += bf2f(xr[i]);
  red[t] = s; __syncthreads();
  if (t == 0) { float a = 0; for (int i = 0; i < 64; ++i) a += red[i]; sm = a / D_; }
  __syncthreads();
  float mean = sm;
  float q = 0;
  for (int i = t; i < D_; i += 64) { float d = bf2f(xr[i]) - mean; q += d * d; }
  red[t] = q; __syncthreads();
  if (t == 0) { float a = 0; for (int i = 0; i < 64; ++i) a += red[i]; sv = a / D_; }
  __syncthreads();
  float mb = rbf(mean);
  float vb = rbf(sv);
  float den = rbf(sqrtf(rbf(vb + 1e-5f)));
  for (int i = t; i < D_; i += 64) {
    float d = rbf(bf2f(xr[i]) - mb);
    xn[row * D_ + i] = f2bf(d / den);
  }
}

__global__ void n_kv(const u16* k, const u16* v, const u16* sk, u16* kv) {
  int idx = blockIdx.x * blockDim.x + threadIdx.x;
  if (idx >= S_ * D_) return;
  int t = idx / D_, d = idx % D_;
  float acc = 0.f;
  for (int j = 0; j < 4; ++j) {
    int tt = t - 3 + j;
    float kk = tt >= 0 ? bf2f(k[tt * D_ + d]) : 0.f;
    float p = rbf(kk * bf2f(sk[d * 4 + 3 - j]));
    acc = rbf(acc + p);
  }
  kv[idx] = f2bf(acc * bf2f(v[idx]));
}

__global__ void n_conv(const u16* kv, const u16* ssk, const u16* sD, u16* y) {
  int idx = blockIdx.x * blockDim.x + threadIdx.x;
  if (idx >= S_ * D_) return;
  int t = idx / D_, d = idx % D_;
  float acc = 0.f;
  for (int s = 0; s <= t; ++s) acc += bf2f(kv[s * D_ + d]) * bf2f(ssk[d * S_ + (t - s)]);
  float yb = rbf(acc);
  float p = rbf(bf2f(kv[idx]) * bf2f(sD[d]));
  y[idx] = f2bf(yb + p);
}

__global__ void n_g1(const u16* xn, const u16* w1, const u16* b1, u16* h) {
  int idx = blockIdx.x * blockDim.x + threadIdx.x;
  if (idx >= S_ * F_) return;
  int t = idx / F_, f = idx % F_;
  float acc = 0.f;
  for (int k = 0; k < D_; ++k) acc += bf2f(xn[t * D_ + k]) * bf2f(w1[k * F_ + f]);
  float hh = rbf(rbf(acc) + bf2f(b1[f]));
  float r = hh > 0.f ? hh : 0.f;
  h[idx] = f2bf(r * r);
}

__global__ void n_g2(const u16* h, const u16* w2, const u16* b2, const u16* x, const u16* xn, const u16* y, u16* out) {
  int idx = blockIdx.x * blockDim.x + threadIdx.x;
  if (idx >= S_ * D_) return;
  int t = idx / D_, d = idx % D_;
  float acc = 0.f;
  for (int k = 0; k < F_; ++k) acc += bf2f(h[t * F_ + k]) * bf2f(w2[k * D_ + d]);
  float o = rbf(rbf(acc) + bf2f(b2[d]));
  float g = rbf(o / (1.f + expf(-o)));
  float s = rbf(bf2f(xn[idx]) * bf2f(y[idx]));
  float sg = rbf(s * g);
  out[idx] = f2bf(bf2f(x[idx]) + sg);
}

extern "C" void kernel_launch(void* const* d_in, const int* in_sizes, int n_in,
                              void* d_out, int out_size, void* d_ws, size_t ws_size,
                              hipStream_t stream) {
  const u16* q = (const u16*)d_in[0];
  const u16* k = (const u16*)d_in[1];
  const u16* v = (const u16*)d_in[2];
  const u16* sk = (const u16*)d_in[3];
  const u16* ssk = (const u16*)d_in[4];
  const u16* sD = (const u16*)d_in[5];
  const u16* w1 = (const u16*)d_in[7];
  const u16* b1 = (const u16*)d_in[8];
  const u16* w2 = (const u16*)d_in[9];
  const u16* b2 = (const u16*)d_in[10];
  u16* out = (u16*)d_out;
  u16* xn = (u16*)d_ws;
  u16* kv = xn + S_ * D_;
  u16* y = kv + S_ * D_;
  u16* h = y + S_ * D_;
  n_ln<<<S_, 64, 0, stream>>>(q, xn);
  n_kv<<<(S_ * D_ + 255) / 256, 256, 0, stream>>>(k, v, sk, kv);
  n_conv<<<(S_ * D_ + 255) / 256, 256, 0, stream>>>(kv, ssk, sD, y);
  n_g1<<<(S_ * F_ + 255) / 256, 256, 0, stream>>>(xn, w1, b1, h);
  n_g2<<<(S_ * D_ + 255) / 256, 256, 0, stream>>>(h, w2, b2, q, xn, y, out);
}
```

```cpp
#include <hip/hip_runtime.h>
#include <hip/hip_bf16.h>
#include <stdint.h>

typedef unsigned short u16;
typedef unsigned long long u64;
typedef __bf16 bf16x8 __attribute__((ext_vector_type(8)));
typedef __bf16 bf16x2 __attribute__((ext_vector_type(2)));
typedef short s16x4 __attribute__((ext_vector_type(4)));
typedef short s16x8 __attribute__((ext_vector_type(8)));
typedef float f32x2 __attribute__((ext_vector_type(2)));
typedef float f32x4 __attribute__((ext_vector_type(4)));
typedef float f32x16 __attribute__((ext_vector_type(16)));
typedef unsigned u32x2 __attribute__((ext_vector_type(2)));
typedef unsigned u32x4 __attribute__((ext_vector_type(4)));
typedef int i32x4 __attribute__((ext_vector_type(4)));
#define GAS __attribute__((address_space(1)))
#define LAS __attribute__((address_space(3)))
#define DI __device__ __forceinline__

constexpr int S_ = 2048, D_ = 448, F_ = 1792;
constexpr int NWG = 224;
constexpr unsigned FSTR = 256;
constexpr unsigned WS_EPOCH = 0;
constexpr unsigned WS_KVF = 4096;
constexpr unsigned WS_HF = WS_KVF + 1792 * FSTR;
constexpr unsigned WS_YF = WS_HF + 1792 * FSTR;
constexpr unsigned WS_KVT = 2u << 20;
constexpr unsigned WS_YT = 4u << 20;
constexpr unsigned WS_HT = 6u << 20;
constexpr unsigned WS_END = WS_HT + 64u * 112u * 1024u;
constexpr unsigned WS_DBG = 16u << 20;
constexpr unsigned L_X = 0;
constexpr unsigned L_W = 49152;
constexpr unsigned L_E = 98304;
constexpr unsigned E_SZ = 16640;
constexpr unsigned U_PL = 1568, U_SZ = 4 * U_PL;
constexpr unsigned L_U = L_E + 2 * E_SZ;
constexpr unsigned L_KV = L_U + 2 * U_SZ;
constexpr unsigned L_SP = L_KV;
constexpr unsigned L_CS = L_KV + 4096;
constexpr unsigned L_Y = L_KV;
constexpr unsigned L_ST = L_KV + 9216;
constexpr unsigned P_SZ = 8704;
constexpr unsigned L_CP = 0;
constexpr unsigned L_G0 = 69632;
constexpr unsigned L_G1 = 0;
constexpr unsigned LDS_BYTES = L_ST + 1024;
static_assert(L_G0 + 7 * P_SZ <= L_U, "lds");
static_assert(LDS_BYTES <= 163840, "lds");

constexpr unsigned SPIN_CAP = 1u << 18;

DI float bflo(unsigned v) { return __uint_as_float(v << 16); }
DI float bfhi(unsigned v) { return __uint_as_float(v & 0xffff0000u); }
DI unsigned pk(float lo, float hi) { f32x2 f = {lo, hi}; return __builtin_bit_cast(unsigned, __builtin_convertvector(f, bf16x2)); }
DI float rbf(float a) { return bflo(pk(a, a)); }
DI float dot2(unsigned a, unsigned b, float c) { return __builtin_amdgcn_fdot2_f32_bf16(__builtin_bit_cast(bf16x2, a), __builtin_bit_cast(bf16x2, b), c, false); }
DI f32x16 mfma(u32x4 a, u32x4 b, f32x16 c) { return __builtin_amdgcn_mfma_f32_32x32x16_bf16(__builtin_bit_cast(bf16x8, a), __builtin_bit_cast(bf16x8, b), c, 0, 0, 0); }
DI u32x4 lds128(unsigned a) { return *(const LAS u32x4*)a; }
DI u32x2 lds64(unsigned a) { return *(const LAS u32x2*)a; }
DI u32x2 ldstr(unsigned a) { return __builtin_bit_cast(u32x2, __builtin_amdgcn_ds_read_tr16_b64_v4i16((LAS s16x4*)a)); }
DI void sts128(unsigned a, u32x4 v) { *(LAS u32x4*)a = v; }
DI i32x4 mksrd(const void* p, unsigned bytes) {
  u64 a = (u64)p;
  i32x4 r = {(int)(unsigned)a, (int)((unsigned)(a >> 32) & 0xffffu), (int)bytes, 0x00020000};
  return r;
}
#define VMWAIT(n) asm volatile("s_waitcnt vmcnt(%0)" ::"i"(n) : "memory")
#define BAR() asm volatile("s_waitcnt lgkmcnt(0)\n\ts_barrier" ::: "memory")
#define OPAQUE(x) asm volatile("" : "+v"(x))
#define SB() __builtin_amdgcn_sched_barrier(0)
DI void dma(unsigned ldst, unsigned voff, i32x4 srd, unsigned soff) {
  unsigned keep;
  asm volatile("s_mov_b32 %0, m0\n\ts_mov_b32 m0, %1\n\ts_nop 0\n\tbuffer_load_dwordx4 %2, %3, %4 offen lds\n\ts_mov_b32 m0, %0"
               : "=&s"(keep) : "s"(ldst), "v"(voff), "s"(srd), "s"(soff));
}
template <typename R> DI void st128_sc1(u32x4 v, R rsrc, unsigned voff, unsigned soff) {
  __builtin_amdgcn_raw_buffer_store_b128(v, rsrc, voff, soff, 16);
  asm volatile("s_nop 1" ::"v"(v) : "memory");
}
DI void flag_store(char* ws, unsigned off, u64 tag, int lane) {
  if (lane == 0) __hip_atomic_store((GAS u64*)(ws + off), tag, __ATOMIC_RELAXED, __HIP_MEMORY_SCOPE_AGENT);
}
DI void poll_flags(char* ws, unsigned off, u64 tag) {
#pragma unroll 1
  for (unsigned spins = 0; spins < SPIN_CAP; ++spins) {
    u64 f = __hip_atomic_load((GAS u64*)(ws + off), __ATOMIC_RELAXED, __HIP_MEMORY_SCOPE_AGENT);
    if (__all(f == tag)) break;
    __builtin_amdgcn_s_sleep(1);
  }
  asm volatile("" ::: "memory");
}

#define STAMP(i) do {} while (0)

#define PART42 0
#define SIGF 8

__global__ __launch_bounds__(512, 2) void fused_h3_mlp(const u16* __restrict__ gx, const u16* __restrict__ gk, const u16* __restrict__ gv,
                                                      const u16* __restrict__ gshk, const u16* __restrict__ gssk, const u16* __restrict__ gsD,
                                                      const u16* __restrict__ gw1, const u16* __restrict__ gb1, const u16* __restrict__ gw2,
                                                      const u16* __restrict__ gb2, u16* __restrict__ gout, char* __restrict__ ws) {
  STAMP(0);
  const int b = blockIdx.x, g = b & 7, ii = b >> 3;
  const int w = __builtin_amdgcn_readfirstlane(threadIdx.x >> 6);
  const int lane = threadIdx.x & 63, r = lane & 31, hh = lane >> 5;
  if (b >= NWG) {
    if (w == 0) {
      const u64 tg0 = __hip_atomic_load((GAS u64*)(ws + WS_EPOCH), __ATOMIC_RELAXED, __HIP_MEMORY_SCOPE_AGENT) + 1ull;
#pragma unroll 1
      for (int j = 0; j < 28; ++j) poll_flags(ws, WS_HF + (j * 64 + lane) * FSTR, tg0);
      flag_store(ws, WS_EPOCH, tg0, lane);
    }
    return;
  }
  const bool cw = w < 7;
  const int mt128 = 2 * g + ii / 14, ft = ii % 14;
  const int mt64 = 4 * g + ii / 7, nt = ii % 7;
  const int fg = w & 3, tg = w >> 2;
  const int tt = b / 7, cg = b % 7;

  const i32x4 sX = mksrd(gx, S_ * D_ * 2), sW1 = mksrd(gw1, D_ * F_ * 2), sK = mksrd(gk, S_ * D_ * 2), sV = mksrd(gv, S_ * D_ * 2);
  const i32x4 sH = mksrd(gssk, S_ * D_ * 2), sWS = mksrd(ws, WS_END);
  const auto rWS = __builtin_amdgcn_make_buffer_rsrc(ws, 0, WS_END, 0x00020000);
  const auto rW2 = __builtin_amdgcn_make_buffer_rsrc((void*)gw2, 0, D_ * F_ * 2, 0x00020000);

  const unsigned dpair = *(const unsigned*)(gsD + 2 * b);
  const float dch = hh ? bfhi(dpair) : bflo(dpair);
  unsigned vx0, vx1, vw0, vw1;
  {
    int row0 = 16 * w + (lane >> 3), row1 = row0 + 8, pos = lane & 7;
    vx0 = (128 * mt128 + row0) * 896 + ((pos ^ ((row0 >> 1) & 7)) << 4);
    vx1 = (128 * mt128 + row1) * 896 + ((pos ^ ((row1 >> 1) & 7)) << 4);
    int q0 = 8 * w + (lane >> 4), q1 = q0 + 4, p = lane & 15;
    vw0 = q0 * 3584 + ft * 256 + ((p ^ (((q0 & 3) << 2) | ((q0 >> 2) & 3))) << 4);
    vw1 = q1 * 3584 + ft * 256 + ((p ^ (((q1 & 3) << 2) | ((q1 >> 2) & 3))) << 4);
  }
  auto dma1 = [&](int k, int j) {
    const unsigned sl = (k % 3) * 16384 + w * 2048;
    if (j == 0) dma(L_X + sl, vx0, sX, k * 128);
    if (j == 1) dma(L_W + sl, vw0, sW1, k * 64 * 3584);
    if (j == 2) dma(L_X + sl + 1024, vx1, sX, k * 128);
    if (j == 3) dma(L_W + sl + 1024, vw1, sW1, k * 64 * 3584);
  };
#pragma unroll
  for (int j = 0; j < 8; ++j) dma1(j >> 2, j & 3);
  u32x2 ep;
  asm volatile("buffer_load_dwordx2 %0, off, %1, 0 sc1" : "=v"(ep) : "s"(sWS) : "memory");
  unsigned kr[11], vr[8];
  {
    const unsigned l2 = lane * 2;
    const int t0 = 64 * tt + 8 * w;
#pragma unroll
    for (int j = 0; j < 11; ++j) {
      int row = t0 - 3 + j; row = row < 0 ? 0 : row;
      kr[j] = 0;
      asm volatile("buffer_load_short_d16_hi %0, %1, %2, %3 offen" : "+v"(kr[j]) : "v"(l2), "s"(sK), "s"(row * 896 + cg * 128) : "memory");
    }
#pragma unroll
    for (int j = 0; j < 8; ++j) {
      vr[j] = 0;
      asm volatile("buffer_load_short_d16_hi %0, %1, %2, %3 offen" : "+v"(vr[j]) : "v"(l2), "s"(sV), "s"((t0 + j) * 896 + cg * 128) : "memory");
    }
  }
  u32x2 shk;
  asm volatile("global_load_dwordx2 %0, %1, %2" : "=v"(shk) : "v"((cg * 64 + lane) * 8), "s"(gshk) : "memory");
  u32x4 e0; u32x2 e1;
  {
    const int c = 64 * (w & 3) + lane;
    const unsigned so = (2 * b + tg) * 4096;
    asm volatile("buffer_load_dwordx4 %0, %1, %2, %3 offen" : "=v"(e0) : "v"(c * 16), "s"(sH), "s"(so) : "memory");
    asm volatile("buffer_load_dwordx2 %0, %1, %2, %3 offen" : "=v"(e1) : "v"((c == 255 ? 254 : c) * 16 + 16), "s"(sH), "s"(so) : "memory");
  }

  unsigned aA[4][2], aB[4];
  {
    const int rr = fg ^ (2 * tg);
    const int blk = (lane >> 4) & 1, q = (lane & 15) >> 2, p = lane & 3;
#pragma unroll
    for (int j = 0; j < 4; ++j) {
      const int ksl = j ^ rr;
#pragma unroll
      for (int t = 0; t < 2; ++t) {
        const int row = 16 * ksl + 8 * hh + 4 * t + q;
        const int ch = 4 * fg + 2 * blk + (p >> 1);
        aA[j][t] = L_W + 256 * row + 16 * (ch ^ ((q << 2) | ((2 * hh + t) & 3))) + 8 * (p & 1);
      }
      aB[j] = L_X + (64 * tg + r) * 128 + (((2 * ksl + hh) ^ ((r >> 1) & 7)) << 4);
    }
  }
  f32x16 acc0, acc1;
#pragma unroll
  for (int i = 0; i < 16; ++i) { acc0[i] = 0.f; acc1[i] = 0.f; }
  float s0 = 0.f, q0 = 0.f, s1 = 0.f, q1 = 0.f, cs = 0.f;
  const unsigned ONES = 0x3f803f80u;
  auto compute = [&](int k) {
    const unsigned so = (k % 3) * 16384;
    u32x4 A[2], B0[2], B1[2];
    auto ldf = [&](int j) {
      u32x2 a0 = ldstr(aA[j][0] + so), a1 = ldstr(aA[j][1] + so);
      A[j & 1] = u32x4{a0[0], a0[1], a1[0], a1[1]};
      B0[j & 1] = lds128(aB[j] + so); B1[j & 1] = lds128(aB[j] + so + 4096);
    };
    ldf(0);
#pragma unroll
    for (int j = 0; j < 4; ++j) {
      if (j < 3) ldf(j + 1);
      SB();
      acc0 = mfma(A[j & 1], B0[j & 1], acc0);
      acc1 = mfma(A[j & 1], B1[j & 1], acc1);
      asm volatile("" : "+v"(acc0), "+v"(acc1));
      SB();
      if (j == 0) {
#pragma unroll
        for (int e = 0; e < 4; ++e) { s0 = dot2(B0[0][e], ONES, s0); q0 = dot2(B0[0][e], B0[0][e], q0); s1 = dot2(B1[0][e], ONES, s1); q1 = dot2(B1[0][e], B1[0][e], q1); }
      }
      if (j == 1 || j == 2) {
#pragma unroll
        for (int e = 0; e < 4; ++e) cs = dot2(A[j & 1][e], ONES, cs);
      }
      asm volatile("" : "+v"(s0), "+v"(q0), "+v"(s1), "+v"(q1), "+v"(cs));
      SB();
      if (k + 2 < 7) dma1(k + 2, j);
    }
    SB();
  };

  STAMP(1);
  VMWAIT(27); BAR();
  STAMP(2);
  compute(0);
  asm volatile("s_waitcnt vmcnt(4)"
               : "+v"(ep), "+v"(kr[0]), "+v"(kr[1]), "+v"(kr[2]), "+v"(kr[3]), "+v"(kr[4]), "+v"(kr[5]), "+v"(kr[6]), "+v"(kr[7]), "+v"(kr[8]),
                 "+v"(kr[9]), "+v"(kr[10]), "+v"(vr[0]), "+v"(vr[1]), "+v"(vr[2]), "+v"(vr[3]), "+v"(vr[4]), "+v"(vr[5]), "+v"(vr[6]), "+v"(vr[7]),
                 "+v"(shk), "+v"(e0), "+v"(e1)
               :: "memory");
  const u64 tag = (((u64)(unsigned)__builtin_amdgcn_readfirstlane(ep[1]) << 32) | (unsigned)__builtin_amdgcn_readfirstlane(ep[0])) + 1ull;
  {
    if (tt == 0 && w == 0) { kr[0] = 0; kr[1] = 0; kr[2] = 0; }
    const float w0 = bflo(shk[0]), w1 = bfhi(shk[0]), w2 = bflo(shk[1]), w3 = bfhi(shk[1]);
    float kvv[8];
#pragma unroll
    for (int j = 0; j < 8; j += 2) {
      unsigned pa = pk(__uint_as_float(kr[j]) * w3, __uint_as_float(kr[j + 1]) * w3);
      unsigned pb = pk(__uint_as_float(kr[j + 1]) * w2, __uint_as_float(kr[j + 2]) * w2);
      unsigned pc = pk(__uint_as_float(kr[j + 2]) * w1, __uint_as_float(kr[j + 3]) * w1);
      unsigned pd = pk(__uint_as_float(kr[j + 3]) * w0, __uint_as_float(kr[j + 4]) * w0);
      unsigned a = pk(bflo(pa) + bflo(pb), bfhi(pa) + bfhi(pb));
      a = pk(bflo(a) + bflo(pc), bfhi(a) + bfhi(pc));
      a = pk(bflo(a) + bflo(pd), bfhi(a) + bfhi(pd));
      kvv[j] = bflo(a) * __uint_as_float(vr[j]);
      kvv[j + 1] = bfhi(a) * __uint_as_float(vr[j + 1]);
    }
    u32x4 o = {pk(kvv[7], kvv[6]), pk(kvv[5], kvv[4]), pk(kvv[3], kvv[2]), pk(kvv[1], kvv[0])};
    sts128(L_KV + lane * 144 + w * 16, o);
    const unsigned eb = L_E + tg * E_SZ;
    const unsigned d0 = e0[0], d1 = e0[1], d2 = e0[2], d3 = e0[3], d4 = e1[0];
    const unsigned o0 = __builtin_amdgcn_alignbit(d1, d0, 16), o1 = __builtin_amdgcn_alignbit(d2, d1, 16), o2 = __builtin_amdgcn_alignbit(d3, d2, 16),
                   o3 = __builtin_amdgcn_alignbit(d4, d3, 16), o4 = __builtin_amdgcn_alignbit(e1[1], d4, 16);
    const unsigned ea = eb + 256 + (64 * fg + lane) * 64;
    sts128(ea, u32x4{d0, d1, o0, o1});
    sts128(ea + 16, u32x4{d1, d2, o1, o2});
    sts128(ea + 32, u32x4{d2, d3, o2, o3});
    sts128(ea + 48, u32x4{d3, d4, o3, o4});
    if (w < 4) sts128(L_U + (w >> 1) * U_SZ + ((w & 1) * 2 + hh) * U_PL + r * 16, u32x4{0u, 0u, 0u, 0u});
    if (fg == 0) {
      const unsigned h0 = __builtin_amdgcn_readfirstlane(d0), h1 = __builtin_amdgcn_readfirstlane(o0);
      u32x4 z = {0u, 0u, 0u, 0u};
      if (lane == 14) z[3] = h0 << 16;
      if (lane == 15) { z[1] = h0; z[2] = h0 << 16; z[3] = h1; }
      if (lane < 16) sts128(eb + lane * 16, z);
    }
  }
  BAR();
  STAMP(3);
  {
    u32x4 o = lds128(L_KV + (8 * w + (lane >> 3)) * 144 + (lane & 7) * 16);
    st128_sc1(o, rWS, (64 * cg + 8 * w + (lane >> 3)) * 4096 + (lane & 7) * 16, WS_KVT + tt * 128);
    asm volatile("" ::: "memory");
  }
  compute(1);
  VMWAIT(5); BAR();
  STAMP(4);
  compute(2);
  VMWAIT(4);
  flag_store(ws, WS_KVF + ((cg * 32 + tt) * 8 + w) * FSTR, tag, lane);
  BAR();
  STAMP(5);
  compute(3);
  VMWAIT(5); BAR();
  STAMP(6);
  compute(4);
  u32x2 b1r[4];
#pragma unroll
  for (int g4 = 0; g4 < 4; ++g4)
    asm volatile("global_load_dwordx2 %0, %1, %2 offset:%3" : "=v"(b1r[g4]) : "v"((128 * ft + 32 * fg + 4 * hh) * 2), "s"(gb1), "i"(g4 * 16) : "memory");
  VMWAIT(8); BAR();
  STAMP(7);
  const unsigned kvf_off = WS_KVF + (((b >> 5) * 32 + 8 * fg + (lane & 7)) * 8 + ((b >> 2) & 7)) * FSTR;
  u32x2 pf;
  asm volatile("buffer_load_dwordx2 %0, %1, %2, 0 offen sc1" : "=v"(pf) : "v"(kvf_off), "s"(sWS) : "memory");
  compute(5);
  asm volatile("s_waitcnt vmcnt(0)" : "+v"(pf), "+v"(b1r[0]), "+v"(b1r[1]), "+v"(b1r[2]), "+v"(b1r[3]) :: "memory");
  if (!__all((((u64)pf[1] << 32) | pf[0]) == tag)) poll_flags(ws, kvf_off, tag);
  u32x4 uld;
  asm volatile("buffer_load_dwordx4 %0, %1, %2, %3 offen sc1" : "=v"(uld) : "v"(lane * 16), "s"(sWS), "s"(WS_KVT + (2 * b + tg) * 4096 + fg * 1024) : "memory");
  BAR();
  STAMP(8);
  unsigned wr[16][8];
  const unsigned w2v = (unsigned)(hh * 4 * 896 + nt * 128 + r * 4);
  auto ldw2 = [&](int n0, int n1) {
    if (cw) {
#pragma unroll
      for (int n = n0; n < n1; ++n) {
        const int ks = n >> 3, j = n & 7;
        wr[ks][j] = __builtin_amdgcn_raw_buffer_load_b32(rW2, w2v + (j & 3) * 896, (256 * w + 16 * ks + 8 * (j >> 2)) * 896, 0);
      }
    }
    SB();
  };
  ldw2(0, 16);
  compute(6);
  {
    s0 += __shfl_xor(s0, 32); q0 += __shfl_xor(q0, 32); s1 += __shfl_xor(s1, 32); q1 += __shfl_xor(q1, 32); cs += __shfl_xor(cs, 32);
    f32x2 sp = {hh ? s1 : s0, hh ? q1 : q0};
    *(LAS f32x2*)(L_SP + (64 * tg + lane) * 32 + fg * 8) = sp;
    if (hh == 0) *(LAS float*)(L_CS + tg * 512 + (32 * fg + r) * 4) = cs;
  }
  if (!cw) VMWAIT(0);
  asm volatile("s_waitcnt vmcnt(16)" : "+v"(uld) :: "memory");
  {
    const int n = 64 * fg + lane;
    sts128(L_U + tg * U_SZ + (n & 3) * U_PL + ((n >> 2) + 32) * 16, uld);
  }
  BAR();
  STAMP(9);
  ldw2(16, 24);
  {
    float c1[2], rd[2];
#pragma unroll
    for (int t = 0; t < 2; ++t) {
      const unsigned a = L_SP + (64 * tg + 32 * t + r) * 32;
      f32x4 p0 = *(const LAS f32x4*)a, p1 = *(const LAS f32x4*)(a + 16);
      const float sum = (p0[0] + p0[2]) + (p1[0] + p1[2]), sq = (p0[1] + p0[3]) + (p1[1] + p1[3]);
      const float mean = sum * (1.f / D_);
      const float var = fmaxf(sq * (1.f / D_) - mean * mean, 0.f);
      const float mb = rbf(mean);
      const float den = rbf(__builtin_amdgcn_sqrtf(rbf(rbf(var) + 1e-5f)));
      rd[t] = __builtin_amdgcn_rcpf(den);
      c1[t] = -mb * rd[t];
      if (fg == 0 && hh == 0) *(LAS f32x2*)(L_ST + (64 * tg + 32 * t + r) * 8) = f32x2{mb, rd[t]};
    }
#pragma unroll
    for (int s = 0; s < 2; ++s) {
      u32x4 h0, h1;
#pragma unroll
      for (int gg = 0; gg < 2; ++gg) {
        const int g4 = 2 * s + gg;
        const unsigned ca = L_CS + (32 * fg + 8 * g4 + 4 * hh) * 4;
        f32x4 c = *(const LAS f32x4*)ca + *(const LAS f32x4*)(ca + 512);
        const float bb[4] = {bflo(b1r[g4][0]), bfhi(b1r[g4][0]), bflo(b1r[g4][1]), bfhi(b1r[g4][1])};
        float v0[4], v1[4];
#pragma unroll
        for (int e = 0; e < 4; ++e) {
          float t0 = fmaxf(fmaf(acc0[4 * g4 + e], rd[0], fmaf(c1[0], c[e], bb[e])), 0.f);
          float t1 = fmaxf(fmaf(acc1[4 * g4 + e], rd[1], fmaf(c1[1], c[e], bb[e])), 0.f);
          v0[e] = t0 * t0; v1[e] = t1 * t1;
        }
        h0[2 * gg] = pk(v0[0], v0[1]); h0[2 * gg + 1] = pk(v0[2], v0[3]);
        h1[2 * gg] = pk(v1[0], v1[1]); h1[2 * gg + 1] = pk(v1[2], v1[3]);
      }
      const unsigned so = WS_HT + ((4 * mt128 + 2 * tg) * 112 + 8 * ft + 2 * fg + s) * 1024;
      st128_sc1(h0, rWS, lane * 16, so);
      st128_sc1(h1, rWS, lane * 16, so + 112 * 1024);
    }
  }
  STAMP(10);
  {
    f32x16 c0, c1;
#pragma unroll
    for (int i = 0; i < 16; ++i) { c0[i] = 0.f; c1[i] = 0.f; }
    unsigned ea0 = L_E + tg * E_SZ + 8 * (32 + r - 8 * hh - 7 + 256 * fg - 16), ea1 = ea0 + 32, ea2 = ea0 + 8192, ea3 = ea0 + 8224;
    unsigned ua = L_U + tg * U_SZ + hh * U_PL + 16 * (r + 32 - 8 * fg - 7);
    OPAQUE(ea0); OPAQUE(ea1); OPAQUE(ea2); OPAQUE(ea3); OPAQUE(ua);
    u32x4 A0[2], A1[2], B0[2], B1[2];
    auto ldc = [&](int st) {
      const int dd = st >> 1, s = st & 1;
      const unsigned eo = 8 * (32 * dd + 16 * (1 - s)), uo = 2 * s * U_PL + 16 * (7 - dd);
      u32x2 x0 = lds64(ea0 + eo), x1 = lds64(ea1 + eo);
      B0[st & 1] = lds128(ua + uo); B1[st & 1] = lds128(ua + uo + 512);
      u32x2 y0 = lds64(ea2 + eo), y1 = lds64(ea3 + eo);
      A0[st & 1] = u32x4{x0[0], x0[1], x1[0], x1[1]}; A1[st & 1] = u32x4{y0[0], y0[1], y1[0], y1[1]};
    };
    ldc(0);
    SB();
#pragma unroll
    for (int st = 0; st < 16; ++st) {
      if (st + 1 < 16) ldc(st + 1);
      SB();
      c0 = mfma(A0[st & 1], B0[st & 1], c0);
      c1 = mfma(A0[st & 1], B1[st & 1], c1);
      c1 = mfma(A1[st & 1], B0[st & 1], c1);
      asm volatile("" : "+v"(c0), "+v"(c1));
      if (st == SIGF) {
        if (cw) VMWAIT(7 * SIGF); else VMWAIT(0);
        flag_store(ws, WS_HF + ((2 * mt128 + tg) * 56 + 4 * ft + fg) * FSTR, tag, lane);
      }
      if (st < 8) ldw2(24 + 7 * st, 31 + 7 * st); else ldw2(80 + 6 * (st - 8), 86 + 6 * (st - 8));
    }
    STAMP(12);
    const unsigned pb = L_CP + w * P_SZ + lane * 16;
#pragma unroll
    for (int g4 = 0; g4 < 4; ++g4) {
      *(LAS f32x4*)(pb + g4 * 1088) = f32x4{c0[4 * g4], c0[4 * g4 + 1], c0[4 * g4 + 2], c0[4 * g4 + 3]};
      *(LAS f32x4*)(pb + 4352 + g4 * 1088) = f32x4{c1[4 * g4], c1[4 * g4 + 1], c1[4 * g4 + 2], c1[4 * g4 + 3]};
    }
  }
  BAR();
  {
    const int n = 32 * w + r, a = n >> 2, g4 = n & 3;
    const unsigned po = L_CP + hh * 4 * P_SZ + (a >> 5) * 4352 + g4 * 1088 + (a & 31) * 16;
    f32x4 lo = {0.f, 0.f, 0.f, 0.f}, hi = {0.f, 0.f, 0.f, 0.f};
#pragma unroll
    for (int q = 0; q < 4; ++q) {
      lo += *(const LAS f32x4*)(po + q * P_SZ);
      hi += *(const LAS f32x4*)(po + q * P_SZ + 512);
    }
    u32x4 kvc = lds128(L_U + hh * U_SZ + g4 * U_PL + (a + 32) * 16);
    u32x4 yo;
    {
      unsigned cq = pk(lo[0], lo[1]), pq = pk(bfhi(kvc[3]) * dch, bflo(kvc[3]) * dch);
      yo[0] = pk(bflo(cq) + bflo(pq), bfhi(cq) + bfhi(pq));
      cq = pk(lo[2], lo[3]); pq = pk(bfhi(kvc[2]) * dch, bflo(kvc[2]) * dch);
      yo[1] = pk(bflo(cq) + bflo(pq), bfhi(cq) + bfhi(pq));
      cq = pk(hi[0], hi[1]); pq = pk(bfhi(kvc[1]) * dch, bflo(kvc[1]) * dch);
      yo[2] = pk(bflo(cq) + bflo(pq), bfhi(cq) + bfhi(pq));
      cq = pk(hi[2], hi[3]); pq = pk(bfhi(kvc[0]) * dch, bflo(kvc[0]) * dch);
      yo[3] = pk(bflo(cq) + bflo(pq), bfhi(cq) + bfhi(pq));
    }
    st128_sc1(yo, rWS, (2 * b + hh) * 4096 + n * 16, WS_YT);
    asm volatile("" ::: "memory");
  }
  STAMP(13);
  const int tok = threadIdx.x >> 4, quad = threadIdx.x & 15;
  const u32x2 xr0 = *(const GAS u32x2*)(gx + (64 * mt64 + tok) * D_ + 64 * nt + 4 * quad);
  const u32x2 xr1 = *(const GAS u32x2*)(gx + (64 * mt64 + 32 + tok) * D_ + 64 * nt + 4 * quad);
  const u32x2 b2r = *(const GAS u32x2*)(gb2 + 64 * nt + 4 * quad);
  const unsigned yflag = WS_YF + (((b >> 5) * 8 + w) * 32 + (b & 31)) * FSTR;
  const unsigned wpo = hh * 544 + r * 16;
  const unsigned rpo = (quad >> 2) * 1088 + ((quad >> 1) & 1) * 544 + tok * 16 + (quad & 1) * 8;
  auto gate = [&](unsigned base, float (&gt)[4]) {
    unsigned ra = base + rpo;
    OPAQUE(ra);
    f32x2 ev = *(const LAS f32x2*)ra, od = *(const LAS f32x2*)(ra + 4352);
#pragma unroll
    for (int q = 1; q < 7; ++q) {
      ev += *(const LAS f32x2*)(ra + q * P_SZ);
      od += *(const LAS f32x2*)(ra + q * P_SZ + 4352);
    }
    const unsigned oa = pk(ev[0], od[0]), ob = pk(ev[1], od[1]);
    const unsigned o2 = pk(bflo(oa) + bflo(b2r[0]), bfhi(oa) + bfhi(b2r[0])), o3 = pk(bflo(ob) + bflo(b2r[1]), bfhi(ob) + bfhi(b2r[1]));
    const float o[4] = {bflo(o2), bfhi(o2), bflo(o3), bfhi(o3)};
    float sv[4];
#pragma unroll
    for (int e = 0; e < 4; ++e) sv[e] = o[e] * __builtin_amdgcn_rcpf(1.f + __expf(-o[e]));
    const unsigned ga = pk(sv[0], sv[1]), gb = pk(sv[2], sv[3]);
    gt[0] = bflo(ga); gt[1] = bfhi(ga); gt[2] = bflo(gb); gt[3] = bfhi(gb);
  };
  const unsigned hf_off = WS_HF + (mt64 * 56 + 8 * w + (lane & 7)) * FSTR;
  u32x2 hf;
  asm volatile("buffer_load_dwordx2 %0, %1, %2, 0 offen sc1" : "=v"(hf) : "v"(hf_off), "s"(sWS) : "memory");
  u32x4 we[16], wo[16];
#pragma unroll
  for (int ks = 0; ks < 16; ++ks) {
#pragma unroll
    for (int j = 0; j < 4; ++j) {
      we[ks][j] = __builtin_amdgcn_perm(wr[ks][2 * j + 1], wr[ks][2 * j], 0x05040100u);
      wo[ks][j] = __builtin_amdgcn_perm(wr[ks][2 * j + 1], wr[ks][2 * j], 0x07060302u);
    }
    asm volatile("" : "+v"(we[ks]), "+v"(wo[ks]));
  }
  float g0[4], g1[4];
  if (cw) {
    asm volatile("s_waitcnt vmcnt(0)" : "+v"(hf) :: "memory");
    flag_store(ws, yflag, tag, lane);
    if (!__all((((u64)hf[1] << 32) | hf[0]) == tag)) poll_flags(ws, hf_off, tag);
    STAMP(14);
    constexpr int WIN = 16;
    u32x4 hb[WIN];
    const unsigned hso = WS_HT + (2 * mt64 * 112 + 16 * w) * 1024;
    auto ldh = [&](int n) { return __builtin_amdgcn_raw_buffer_load_b128(rWS, lane * 16 + (n & 3) * 1024, hso + (n >> 4) * 112 * 1024 + (n & 12) * 1024, 16); };
#pragma unroll
    for (int n = 0; n < WIN; ++n) hb[n] = ldh(n);
    SB();
    f32x16 ce, co;
#pragma unroll
    for (int mt = 0; mt < 2; ++mt) {
#pragma unroll
      for (int i = 0; i < 16; ++i) { ce[i] = 0.f; co[i] = 0.f; }
#pragma unroll
      for (int ks = 0; ks < 16; ++ks) {
        const int n = 16 * mt + ks;
        ce = mfma(we[ks], hb[n % WIN], ce);
        co = mfma(wo[ks], hb[n % WIN], co);
        asm volatile("" : "+v"(ce), "+v"(co));
        if (n + WIN < 32) hb[n % WIN] = ldh(n + WIN);
        SB();
      }
      unsigned pb = (mt ? L_G1 : L_G0) + w * P_SZ + wpo;
      OPAQUE(pb);
#pragma unroll
      for (int g4 = 0; g4 < 4; ++g4) {
        *(LAS f32x4*)(pb + g4 * 1088) = f32x4{ce[4 * g4], ce[4 * g4 + 1], ce[4 * g4 + 2], ce[4 * g4 + 3]};
        *(LAS f32x4*)(pb + 4352 + g4 * 1088) = f32x4{co[4 * g4], co[4 * g4 + 1], co[4 * g4 + 2], co[4 * g4 + 3]};
      }
      if (mt == 0) { STAMP(15); BAR(); STAMP(16); gate(L_G0, g0); SB(); }
    }
    STAMP(17);
  } else {
    VMWAIT(0);
    flag_store(ws, yflag, tag, lane);
    STAMP(14);
    STAMP(15);
    BAR();
    STAMP(16);
    gate(L_G0, g0);
    poll_flags(ws, WS_YF + ((nt * 8 + (mt64 >> 2)) * 32 + r) * FSTR, tag);
    STAMP(17);
    u32x4 yb[8];
#pragma unroll
    for (int j = 0; j < 8; ++j) yb[j] = __builtin_amdgcn_raw_buffer_load_b128(rWS, (lane >> 3) * 4096 + (lane & 7) * 16, WS_YT + (64 * nt + 8 * j) * 4096 + mt64 * 128, 16);
#pragma unroll
    for (int j = 0; j < 8; ++j) {
      const unsigned ya = L_Y + (8 * j + (lane >> 3)) * 132 + (lane & 7) * 16;
#pragma unroll
      for (int e = 0; e < 4; ++e) *(LAS unsigned*)(ya + 4 * e) = yb[j][e];
    }
  }
  BAR();
  STAMP(18);
  gate(L_G1, g1);
  {
    const unsigned sta = L_ST + (64 * (mt64 & 1) + tok) * 8;
#pragma unroll
    for (int mt = 0; mt < 2; ++mt) {
      const f32x2 st = *(const LAS f32x2*)(sta + mt * 256);
      const u32x2 xr = mt ? xr1 : xr0;
      const float xv[4] = {bflo(xr[0]), bfhi(xr[0]), bflo(xr[1]), bfhi(xr[1])};
      float yv[4];
#pragma unroll
      for (int e = 0; e < 4; ++e) yv[e] = bflo(*(const LAS u16*)(L_Y + (4 * quad + e) * 132 + (32 * mt + tok) * 2));
      const unsigned da = pk(xv[0] - st[0], xv[1] - st[0]), db = pk(xv[2] - st[0], xv[3] - st[0]);
      const unsigned na = pk(bflo(da) * st[1], bfhi(da) * st[1]), nb = pk(bflo(db) * st[1], bfhi(db) * st[1]);
      const unsigned sa = pk(bflo(na) * yv[0], bfhi(na) * yv[1]), sb = pk(bflo(nb) * yv[2], bfhi(nb) * yv[3]);
      const float* gg = mt ? g1 : g0;
      const unsigned ma = pk(bflo(sa) * gg[0], bfhi(sa) * gg[1]), mb = pk(bflo(sb) * gg[2], bfhi(sb) * gg[3]);
      u32x2 o = {pk(xv[0] + bflo(ma), xv[1] + bfhi(ma)), pk(xv[2] + bflo(mb), xv[3] + bfhi(mb))};
      *(GAS u32x2*)(gout + (64 * mt64 + 32 * mt + tok) * D_ + 64 * nt + 4 * quad) = o;
    }
  }
}

extern "C" void kernel_launch(void* const* d_in, const int* in_sizes, int n_in, void* d_out, int out_size, void* d_ws, size_t ws_size,
                              hipStream_t stream) {
  (void)in_sizes; (void)n_in; (void)out_size; (void)ws_size;
  (void)hipFuncSetAttribute((const void*)fused_h3_mlp, hipFuncAttributeMaxDynamicSharedMemorySize, LDS_BYTES);
  fused_h3_mlp<<<NWG + 1, 512, LDS_BYTES, stream>>>((const u16*)d_in[0], (const u16*)d_in[1], (const u16*)d_in[2], (const u16*)d_in[3],
                                                (const u16*)d_in[4], (const u16*)d_in[5], (const u16*)d_in[7], (const u16*)d_in[8],
                                                (const u16*)d_in[9], (const u16*)d_in[10], (u16*)d_out, (char*)d_ws);
}
```

```cpp
#include <hip/hip_runtime.h>
#include <hip/hip_bf16.h>
#include <stdint.h>
#include <type_traits>

typedef unsigned short u16;
typedef unsigned long long u64;
typedef __bf16 bf16x8 __attribute__((ext_vector_type(8)));
typedef __bf16 bf16x2 __attribute__((ext_vector_type(2)));
typedef short s16x2 __attribute__((ext_vector_type(2)));
typedef short s16x4 __attribute__((ext_vector_type(4)));
typedef float f32x2 __attribute__((ext_vector_type(2)));
typedef float f32x4 __attribute__((ext_vector_type(4)));
typedef float f32x16 __attribute__((ext_vector_type(16)));
typedef unsigned u32x2 __attribute__((ext_vector_type(2)));
typedef unsigned u32x4 __attribute__((ext_vector_type(4)));
typedef int i32x4 __attribute__((ext_vector_type(4)));
#define GAS __attribute__((address_space(1)))
#define LAS __attribute__((address_space(3)))
#define DI __device__ __forceinline__
#pragma clang diagnostic ignored "-Wint-to-pointer-cast"

constexpr int S_ = 2048, D_ = 448, F_ = 1792;
constexpr int NWG = 224;
constexpr unsigned FSTR = 256;
constexpr unsigned WS_EPOCH = 0;
constexpr unsigned WS_KVF = 4096;
constexpr unsigned WS_HF = WS_KVF + 1792 * FSTR;
constexpr unsigned WS_YF = WS_HF + 1792 * FSTR;
constexpr unsigned WS_STG = WS_YF + 1792 * FSTR;
constexpr unsigned WS_CSG = WS_STG + 2048 * 8;
constexpr unsigned WS_KVT = 2u << 20;
constexpr unsigned WS_YT = 4u << 20;
constexpr unsigned WS_HT = 6u << 20;
constexpr unsigned WS_W2T = WS_HT + 64u * 112u * 1024u;
constexpr unsigned WS_END = WS_W2T + 7u * 112u * 2048u;
static_assert(WS_CSG + 1792 * 8 <= WS_KVT, "ws");
constexpr unsigned WS_DBG = 16u << 20;
constexpr unsigned L_X = 49152;
constexpr unsigned L_W = 0;
constexpr unsigned L_E = 98304;
constexpr unsigned E_SZ = 16640;
constexpr unsigned U_PL = 1568, U_SZ = 4 * U_PL;
constexpr unsigned L_U = L_E + 2 * E_SZ;
constexpr unsigned L_KV = L_U + 2 * U_SZ;
constexpr unsigned L_Y = L_KV;
constexpr unsigned L_ST = L_KV + 9216;
constexpr unsigned P_SZ = 8704;
constexpr unsigned L_CP = 0;
constexpr unsigned L_G0 = 69632;
constexpr unsigned L_G1 = 0;
constexpr unsigned L_SK = L_W + 32768;
constexpr unsigned L_SV = L_X + 32768;
constexpr unsigned L_SW = L_SV + 8192;
constexpr unsigned L_SH = L_ST + 1024;
constexpr unsigned L_SS = L_SH + 8192 + 64;
constexpr unsigned G_H = 528, G_G = 1056, G_T = 4224;
constexpr unsigned LDS_BYTES = L_SS + 1024;
static_assert(L_G0 + 7 * P_SZ <= L_U, "lds");
static_assert(LDS_BYTES <= 163840, "lds");

constexpr unsigned SPIN_CAP = 1u << 18;
constexpr unsigned ONES = 0x3f803f80u, ONE_HI = 0x3f800000u;

DI float bflo(unsigned v) { return __uint_as_float(v << 16); }
DI float bfhi(unsigned v) { return __uint_as_float(v & 0xffff0000u); }
DI unsigned pk(float lo, float hi) { f32x2 f = {lo, hi}; return __builtin_bit_cast(unsigned, __builtin_convertvector(f, bf16x2)); }
DI float rbf(float a) { return bfhi(pk(a, a)); }
DI float dot2(unsigned a, unsigned b, float c) { return __builtin_amdgcn_fdot2_f32_bf16(__builtin_bit_cast(bf16x2, a), __builtin_bit_cast(bf16x2, b), c, false); }
DI void dot2x8(float (&d)[8], const unsigned (&a)[8], unsigned b0, unsigned b1, unsigned b2, unsigned b3) {
  asm volatile("v_dot2_f32_bf16 %0, %8, %16, 0\n\tv_dot2_f32_bf16 %1, %9, %16, 0\n\tv_dot2_f32_bf16 %2, %10, %17, 0\n\tv_dot2_f32_bf16 %3, %11, %17, 0\n\t"
               "v_dot2_f32_bf16 %4, %12, %18, 0\n\tv_dot2_f32_bf16 %5, %13, %18, 0\n\tv_dot2_f32_bf16 %6, %14, %19, 0\n\tv_dot2_f32_bf16 %7, %15, %19, 0\n\ts_nop 2"
               : "=&v"(d[0]), "=&v"(d[1]), "=&v"(d[2]), "=&v"(d[3]), "=&v"(d[4]), "=&v"(d[5]), "=&v"(d[6]), "=&v"(d[7])
               : "v"(a[0]), "v"(a[1]), "v"(a[2]), "v"(a[3]), "v"(a[4]), "v"(a[5]), "v"(a[6]), "v"(a[7]), "v"(b0), "v"(b1), "v"(b2), "v"(b3));
}
DI f32x16 mfma(u32x4 a, u32x4 b, f32x16 c) { return __builtin_amdgcn_mfma_f32_32x32x16_bf16(__builtin_bit_cast(bf16x8, a), __builtin_bit_cast(bf16x8, b), c, 0, 0, 0); }
DI u32x4 lds128(unsigned a) { return *(const LAS u32x4*)a; }
DI u32x2 lds64(unsigned a) { return *(const LAS u32x2*)a; }
DI u32x2 ldstr(unsigned a) { return __builtin_bit_cast(u32x2, __builtin_amdgcn_ds_read_tr16_b64_v4i16((LAS s16x4*)a)); }
DI void sts128(unsigned a, u32x4 v) { *(LAS u32x4*)a = v; }
DI i32x4 mksrd(const void* p, unsigned bytes) {
  u64 a = (u64)p;
  i32x4 r = {(int)(unsigned)a, (int)((unsigned)(a >> 32) & 0xffffu), (int)bytes, 0x00020000};
  return r;
}
#define VMWAIT(n) asm volatile("s_waitcnt vmcnt(%0)" ::"i"(n) : "memory")
#define BAR() asm volatile("s_waitcnt lgkmcnt(0)\n\ts_barrier" ::: "memory")
#define OPAQUE(x) asm volatile("" : "+v"(x))
#define SB() __builtin_amdgcn_sched_barrier(0)
#define ROR(x, n) __uint_as_float(__builtin_amdgcn_update_dpp(0u, __float_as_uint(x), 0x120 + (n), 0xf, 0xf, false))
#define PIN8(a) asm volatile("" : "+v"(a[0]), "+v"(a[1]), "+v"(a[2]), "+v"(a[3]), "+v"(a[4]), "+v"(a[5]), "+v"(a[6]), "+v"(a[7]))
template <int LOFF, int GOFF> DI void dma(unsigned wb, unsigned voff, i32x4 srd, unsigned soff) {
  asm volatile("s_add_i32 m0, %0, %4\n\ts_nop 0\n\tbuffer_load_dwordx4 %1, %2, %3 offen offset:%5 lds"
               ::"s"(wb), "v"(voff), "s"(srd), "s"(soff), "i"(LOFF - GOFF), "i"(GOFF) : "scc");
}
template <typename R> DI void st128_sc1(u32x4 v, R rsrc, unsigned voff, unsigned soff) {
  __builtin_amdgcn_raw_buffer_store_b128(v, rsrc, voff, soff, 16);
  asm volatile("s_nop 1" ::"v"(v) : "memory");
}
DI void flag_store(char* ws, unsigned off, u64 tag, int lane) {
  if (lane == 0) __hip_atomic_store((GAS u64*)(ws + off), tag, __ATOMIC_RELAXED, __HIP_MEMORY_SCOPE_AGENT);
}
DI void poll_flags(char* ws, unsigned off, u64 tag) {
#pragma unroll 1
  for (unsigned spins = 0; spins < SPIN_CAP; ++spins) {
    u64 f = __hip_atomic_load((GAS u64*)(ws + off), __ATOMIC_RELAXED, __HIP_MEMORY_SCOPE_AGENT);
    if (__all(f == tag)) break;
    __builtin_amdgcn_s_sleep(1);
  }
  asm volatile("" ::: "memory");
}

DI unsigned poll_gran(char* ws, unsigned off, unsigned tag) {
  u64 f = 0;
#pragma unroll 1
  for (unsigned spins = 0; spins < SPIN_CAP; ++spins) {
    f = __hip_atomic_load((GAS u64*)(ws + off), __ATOMIC_RELAXED, __HIP_MEMORY_SCOPE_AGENT);
    if (__all((unsigned)(f >> 32) == tag)) break;
    __builtin_amdgcn_s_sleep(1);
  }
  asm volatile("" ::: "memory");
  return (unsigned)f;
}

#define STAMP(i) do {} while (0)
#define PART42 0
#define SIGF 8
#define NOW2 0
#define HFIRST 6
#define HWIN 14

__global__ __launch_bounds__(512, 2) void fused_h3_mlp(const u16* __restrict__ gx, const u16* __restrict__ gk, const u16* __restrict__ gv,
                                                      const u16* __restrict__ gshk, const u16* __restrict__ gssk, const u16* __restrict__ gsD,
                                                      const u16* __restrict__ gw1, const u16* __restrict__ gb1, const u16* __restrict__ gw2,
                                                      const u16* __restrict__ gb2, u16* __restrict__ gout, char* __restrict__ ws) {
  STAMP(0);
  const int g = blockIdx.x, yy = blockIdx.y, zz = blockIdx.z;
  const int w = __builtin_amdgcn_readfirstlane(threadIdx.x >> 6);
  const int lane = threadIdx.x & 63, r = lane & 31, hh = lane >> 5;
  const i32x4 sWS = mksrd(ws, WS_END);
  asm volatile("" ::"s"(gx), "s"(gk), "s"(gv), "s"(gshk), "s"(gssk), "s"(gsD), "s"(gw1), "s"(gb1), "s"(gw2), "s"(gb2), "s"(gout), "s"(ws));
  u64 tag;
  if (yy == 7) {
    const int a = g + 8 * zz;
    const i32x4 sX = mksrd(gx, S_ * D_ * 2), sW1 = mksrd(gw1, D_ * F_ * 2);
    u32x4 xr[8], wv[7];
    const int row0 = 256 * g + 64 * zz + 8 * w;
    const unsigned xo = lane < 56 ? lane * 16 : 0x7fffffffu;
#pragma unroll
    for (int j = 0; j < 8; ++j)
      asm volatile("buffer_load_dwordx4 %0, %1, %2, %3 offen" : "=v"(xr[j]) : "v"(xo), "s"(sX), "s"((row0 + j) * 896) : "memory");
    const unsigned wo = a < 28 ? (lane >> 3) * 3584 + (lane & 7) * 16 + a * 128 : 0x7fffffffu;
#pragma unroll
    for (int j = 0; j < 7; ++j)
      asm volatile("buffer_load_dwordx4 %0, %1, %2, %3 offen" : "=v"(wv[j]) : "v"(wo), "s"(sW1), "s"((56 * w + 8 * j) * 3584) : "memory");
    asm volatile("s_load_dwordx2 %0, %1, 0x0\n\ts_waitcnt lgkmcnt(0)" : "=s"(tag) : "s"(ws) : "memory");
    tag += 1ull;
    {
      float v[16];
#pragma unroll
      for (int j = 0; j < 8; ++j) {
        asm volatile("s_waitcnt vmcnt(%1)" : "+v"(xr[j]) : "i"(14 - j) : "memory");
        float s = 0.f, q = 0.f;
#pragma unroll
        for (int e = 0; e < 4; ++e) { s = dot2(xr[j][e], ONES, s); q = dot2(xr[j][e], xr[j][e], q); }
        v[j] = s; v[8 + j] = q;
        asm volatile("" : "+v"(v[j]), "+v"(v[8 + j]));
      }
#pragma unroll
      for (int j = 0; j < 8; ++j) { auto t = __builtin_amdgcn_permlane32_swap(__float_as_uint(v[j]), __float_as_uint(v[8 + j]), false, false); v[j] = __uint_as_float(t[0]) + __uint_as_float(t[1]); }
#pragma unroll
      for (int j = 0; j < 4; ++j) { auto t = __builtin_amdgcn_permlane16_swap(__float_as_uint(v[j]), __float_as_uint(v[4 + j]), false, false); v[j] = __uint_as_float(t[0]) + __uint_as_float(t[1]); }
#pragma unroll
      for (int j = 0; j < 4; ++j) { v[j] += ROR(v[j], 8); v[j] += ROR(v[j], 4); v[j] += ROR(v[j], 2); v[j] += ROR(v[j], 1); }
      const float mine = (lane & 2) ? ((lane & 1) ? v[3] : v[2]) : ((lane & 1) ? v[1] : v[0]);
      const auto t = __builtin_amdgcn_permlane32_swap(__float_as_uint(mine), __float_as_uint(mine), false, false);
      const float sq = __uint_as_float(t[1]);
      const float mean = mine * (1.f / D_);
      const float var = fmaxf(sq * (1.f / D_) - mean * mean, 0.f);
      const float den = __builtin_amdgcn_sqrtf(rbf(rbf(var) + 1e-5f));
      if ((lane & 44) == 0)
        __hip_atomic_store((GAS u64*)(ws + WS_STG + (row0 + ((lane >> 4) & 1) * 4 + (lane & 3)) * 8), ((u64)(unsigned)tag << 32) | pk(mean, den), __ATOMIC_RELAXED, __HIP_MEMORY_SCOPE_AGENT);
    }
    {
      unsigned onelo = 0x3f80u; OPAQUE(onelo);
      float c[8];
#pragma unroll
      for (int e = 0; e < 8; ++e) c[e] = 0.f;
#pragma unroll
      for (int j = 0; j < 7; ++j) {
        asm volatile("s_waitcnt vmcnt(%1)" : "+v"(wv[j]) : "i"(7 - j) : "memory");
#pragma unroll
        for (int e = 0; e < 4; ++e) { c[2 * e] = dot2(wv[j][e], onelo, c[2 * e]); c[2 * e + 1] = dot2(wv[j][e], ONE_HI, c[2 * e + 1]); }
        PIN8(c);
      }
#pragma unroll
      for (int e = 0; e < 4; ++e) { auto t = __builtin_amdgcn_permlane32_swap(__float_as_uint(c[e]), __float_as_uint(c[4 + e]), false, false); c[e] = __uint_as_float(t[0]) + __uint_as_float(t[1]); }
#pragma unroll
      for (int e = 0; e < 2; ++e) { auto t = __builtin_amdgcn_permlane16_swap(__float_as_uint(c[e]), __float_as_uint(c[2 + e]), false, false); c[e] = __uint_as_float(t[0]) + __uint_as_float(t[1]); }
      c[0] += ROR(c[0], 8); c[1] += ROR(c[1], 8);
      *(LAS f32x2*)(w * 256 + (lane & 7) * 32 + (lane >> 4) * 8) = f32x2{c[0], c[1]};
      BAR();
      if (w == 0 && a < 28) {
        float t = 0.f;
#pragma unroll
        for (int j = 0; j < 8; ++j) t += *(const LAS float*)(j * 256 + lane * 4);
        __hip_atomic_store((GAS u64*)(ws + WS_CSG + (64 * a + lane) * 8), ((u64)(unsigned)tag << 32) | __float_as_uint(t), __ATOMIC_RELAXED, __HIP_MEMORY_SCOPE_AGENT);
      }
    }
    if (a == 31) {
#pragma unroll 1
      for (int j = w; j < 28; j += 8) poll_flags(ws, WS_HF + (j * 64 + lane) * FSTR, tag);
#pragma unroll 1
      for (int j = 0; j < 4; ++j) poll_gran(ws, WS_STG + ((w * 4 + j) * 64 + lane) * 8, (unsigned)tag);
      BAR();
      if (w == 0) flag_store(ws, WS_EPOCH, tag, lane);
    }
    return;
  }
  const bool cw = w < 7;
  const int mt128 = 2 * g + (zz >> 1), ft = yy + 7 * (zz & 1), hsel = zz & 1;
  const int mt64 = 2 * mt128 + hsel, nt = yy;
  const int fg = w & 3, tg = w >> 2;
  const int cg = yy, tt = g + 8 * zz;
  const int ch0 = 64 * cg + 2 * tt;
  const bool wu = w < 4 && tt < 28;

  const unsigned wb1 = w * 1024;
  {
    const i32x4 sK = mksrd(gk, S_ * D_ * 2), sV = mksrd(gv, S_ * D_ * 2), sH = mksrd(gssk, S_ * D_ * 2);
    const unsigned vr8 = (lane >> 3) * 896 + (lane & 7) * 16, so = 64 * tt * 896 + cg * 128;
    if (w == 0) dma<L_SK, 0>(0, tt ? vr8 : 0x7fffffffu, sK, tt ? so - 8 * 896 : 0);
    if (w == 1) dma<L_SS, 0>(0, lane * 16, mksrd(gshk, D_ * 8), cg * 512);
    if (tt < 28) dma<L_SW, 0>(wb1, vr8, mksrd(gw2, D_ * F_ * 2), (64 * tt + 8 * w) * 896 + cg * 128);
    dma<L_SK + 1024, 0>(wb1, vr8, sK, so + 8 * w * 896);
    dma<L_SV, 0>(wb1, vr8, sV, so + 8 * w * 896);
    dma<L_SH, 0>(wb1, lane * 16, sH, ch0 * 4096 + wb1);
  }
  const i32x4 sX = mksrd(gx, S_ * D_ * 2), sW1 = mksrd(gw1, D_ * F_ * 2);
  unsigned vx0, vx1, vw0, vw1;
  {
    const int row = 16 * w + (lane >> 3);
    vx0 = (128 * mt128 + row) * 896 + (((lane ^ (lane >> 4)) & 7) << 4);
    vx1 = (vx0 + 8 * 896) ^ 64;
    const int q = lane >> 4;
    vw0 = (8 * w + q) * 3584 + ft * 256 + (((lane & 15) ^ ((q << 2) | (2 * (w & 1)))) << 4);
    vw1 = (vw0 + 4 * 3584) ^ 16;
  }
  const unsigned wb = w * 2048;
#define DMA1(k, j) do { \
    if ((j) == 0) dma<L_X + ((k) % 3) * 16384, (k) * 128>(wb, vx0, sX, 0); \
    if ((j) == 1) dma<L_W + ((k) % 3) * 16384, 0>(wb, vw0, sW1, (k) * 64 * 3584); \
    if ((j) == 2) dma<L_X + ((k) % 3) * 16384 + 1024, (k) * 128>(wb, vx1, sX, 0); \
    if ((j) == 3) dma<L_W + ((k) % 3) * 16384 + 1024, 0>(wb, vw1, sW1, (k) * 64 * 3584); } while (0)
  DMA1(0, 0); DMA1(0, 1); DMA1(0, 2); DMA1(0, 3);
  const auto rWS = __builtin_amdgcn_make_buffer_rsrc(ws, 0, WS_END, 0x00020000);
  unsigned dpair;
  asm volatile("s_load_dwordx2 %0, %2, 0x0\n\ts_load_dword %1, %3, 0x0" : "=&s"(tag), "=&s"(dpair) : "s"(ws), "s"(gsD + ch0) : "memory");

  unsigned aA0, aA1, aB[4];
  const int blk = (lane >> 4) & 1, q4 = (lane & 15) >> 2, p4 = lane & 3;
  {
    const int ch = 4 * fg + 2 * blk + (p4 >> 1);
    aA0 = L_W + 256 * (8 * hh + q4) + 16 * (ch ^ ((q4 << 2) | (2 * hh))) + 8 * (p4 & 1);
    aA1 = (aA0 + 1024) ^ 16;
    const unsigned b0 = L_X + (64 * tg + r) * 128 + ((hh ^ ((r >> 1) & 7)) << 4);
#pragma unroll
    for (int j = 0; j < 4; ++j) { aB[j] = b0 ^ (j << 5); OPAQUE(aB[j]); }
    OPAQUE(aA0); OPAQUE(aA1);
  }
  STAMP(1);
  asm volatile("s_waitcnt vmcnt(4) lgkmcnt(0)\n\ts_barrier" : "+s"(tag), "+s"(dpair) :: "memory");
  tag += 1ull;
  STAMP(2);
  {
    const unsigned ta = (8 * w + q4) * 128 + (lane >> 4) * 32 + p4 * 8;
    const u32x2 k0 = ldstr(L_SK + 512 + ta), k1 = ldstr(L_SK + 1024 + ta), k2 = ldstr(L_SK + 1536 + ta);
    const u32x2 v0 = ldstr(L_SV + ta), v1 = ldstr(L_SV + 512 + ta);
    const u32x2 shk = lds64(L_SS + lane * 8);
    if (wu) {
      const unsigned wa = L_SW + (16 * w + 4 * hh + q4) * 128 + blk * 32 + p4 * 8;
      const u32x2 a0 = ldstr(wa), a1 = ldstr(wa + 1024), b0 = ldstr(wa + 64), b1 = ldstr(wa + 1088);
      const unsigned so = WS_W2T + (cg * 112 + 4 * tt + w) * 2048;
      st128_sc1(u32x4{a0[0], a0[1], a1[0], a1[1]}, rWS, lane * 16, so);
      st128_sc1(u32x4{b0[0], b0[1], b1[0], b1[1]}, rWS, lane * 16 + 1024, so);
    }
    const u32x4 e0 = lds128(L_SH + threadIdx.x * 16);
    const u32x2 e1 = lds64(L_SH + threadIdx.x * 16 + 16);
    DMA1(1, 0);
    const float kr[11] = {bfhi(k0[0]), bflo(k0[1]), bfhi(k0[1]), bflo(k1[0]), bfhi(k1[0]), bflo(k1[1]), bfhi(k1[1]), bflo(k2[0]), bfhi(k2[0]), bflo(k2[1]), bfhi(k2[1])};
    const unsigned vp[4] = {v0[0], v0[1], v1[0], v1[1]};
    const float tw0 = bflo(shk[0]), tw1 = bfhi(shk[0]), tw2 = bflo(shk[1]), tw3 = bfhi(shk[1]);
    float a[8]; unsigned pp[8];
#pragma unroll
    for (int j = 0; j < 8; ++j) pp[j] = pk(kr[j] * tw3, kr[j + 1] * tw2);
    dot2x8(a, pp, ONES, ONES, ONES, ONES); DMA1(1, 1);
#pragma unroll
    for (int j = 0; j < 8; ++j) pp[j] = pk(a[j], kr[j + 2] * tw1);
    dot2x8(a, pp, ONES, ONES, ONES, ONES); DMA1(1, 2);
#pragma unroll
    for (int j = 0; j < 8; ++j) pp[j] = pk(a[j], kr[j + 3] * tw0);
    dot2x8(a, pp, ONES, ONES, ONES, ONES); DMA1(1, 3);
#pragma unroll
    for (int j = 0; j < 8; j += 2) { pp[j] = pk(a[j], 0.f); pp[j + 1] = pk(0.f, a[j + 1]); }
    dot2x8(a, pp, vp[0], vp[1], vp[2], vp[3]);
    sts128(L_KV + lane * 144 + w * 16, u32x4{pk(a[7], a[6]), pk(a[5], a[4]), pk(a[3], a[2]), pk(a[1], a[0])});
    const unsigned d0 = e0[0], d1 = e0[1], d2 = e0[2], d3 = e0[3], d4 = e1[0];
    const unsigned o0 = __builtin_amdgcn_alignbit(d1, d0, 16), o1 = __builtin_amdgcn_alignbit(d2, d1, 16), o2 = __builtin_amdgcn_alignbit(d3, d2, 16),
                   o3 = __builtin_amdgcn_alignbit(d4, d3, 16), o4 = __builtin_amdgcn_alignbit(e1[1], d4, 16);
    const unsigned ea = L_E + 256 + tg * E_SZ + (64 * fg + lane) * 64;
    sts128(ea, u32x4{d0, d1, o0, o1});
    sts128(ea + 16, u32x4{d1, d2, o1, o2});
    sts128(ea + 32, u32x4{d2, d3, o2, o3});
    sts128(ea + 48, u32x4{d3, d4, o3, o4});
    const unsigned h0 = __builtin_amdgcn_readfirstlane(d0), h1 = __builtin_amdgcn_readfirstlane(o0);
    if (fg == 0) {
      u32x4 z = {0u, lane == 15 ? h0 : 0u, lane == 15 ? h0 << 16 : 0u, lane == 15 ? h1 : lane == 14 ? h0 << 16 : 0u};
      if (lane < 16) sts128(L_E + tg * E_SZ + lane * 16, z);
    } else if (fg < 3) {
      sts128(L_U + tg * U_SZ + ((fg - 1) * 2 + hh) * U_PL + r * 16, u32x4{0u, 0u, 0u, 0u});
    }
  }
  f32x16 acc0, acc1;
#pragma unroll
  for (int i = 0; i < 16; ++i) { acc0[i] = 0.f; acc1[i] = 0.f; }
  u32x4 fA[2], fB0[2], fB1[2];
  auto ldf = [&](int n) {
    const int j = n & 3; const unsigned so = ((n >> 2) % 3) * 16384;
    u32x2 a0 = ldstr(aA0 + so + j * 4096), a1 = ldstr(aA1 + so + j * 4096);
    fA[n & 1] = u32x4{a0[0], a0[1], a1[0], a1[1]};
    fB0[n & 1] = lds128(aB[j] + so); fB1[n & 1] = lds128(aB[j] + so + 4096);
  };
#define STEP(n) do { \
    if ((n) + 1 < 28) ldf((n) + 1); \
    SB(); \
    acc0 = mfma(fA[(n) & 1], fB0[(n) & 1], acc0); \
    acc1 = mfma(fA[(n) & 1], fB1[(n) & 1], acc1); \
    asm volatile("" : "+v"(acc0), "+v"(acc1)); \
    SB(); \
    if ((n) + 1 < 20) DMA1((((n) + 1) >> 2) + 2, ((n) + 1) & 3); \
  } while (0)
  if (wu) VMWAIT(6); else VMWAIT(4);
  BAR();
  STAMP(3);
  {
    const u32x4 o = lds128(L_KV + (8 * w + (lane >> 3)) * 144 + (lane & 7) * 16);
    ldf(0);
    DMA1(2, 0);
    st128_sc1(o, rWS, (64 * cg + 8 * w + (lane >> 3)) * 4096 + (lane & 7) * 16, WS_KVT + tt * 128);
  }
  STEP(0); STEP(1); STEP(2);
  VMWAIT(5); BAR();
  STAMP(4);
  STEP(3); STEP(4); STEP(5); STEP(6);
  VMWAIT(4);
  flag_store(ws, WS_KVF + ((cg * 32 + tt) * 8 + w) * FSTR, tag, lane);
  BAR();
  STAMP(5);
  STEP(7); STEP(8); STEP(9); STEP(10);
  VMWAIT(5); BAR();
  STAMP(6);
  STEP(11); STEP(12); STEP(13); STEP(14);
  VMWAIT(4); BAR();
  STAMP(7);
  STEP(15);
  const unsigned kvf_off = WS_KVF + (lane < 8 || !cw ? (cg * 32 + 8 * fg + (lane & 7)) * 8 + (tt >> 2) : (cg * 32 + 4 * w + ((lane >> 2) & 3)) * 8 + (lane & 3)) * FSTR;
  u32x2 pf; unsigned b1v = 0;
  asm volatile("buffer_load_dwordx2 %0, %1, %2, 0 offen sc1" : "=v"(pf) : "v"(kvf_off), "s"(sWS) : "memory");
  asm volatile("global_load_short_d16_hi %0, %1, %2" : "+v"(b1v) : "v"((128 * ft + 32 * fg + r) * 2), "s"(gb1) : "memory");
  STEP(16); STEP(17); STEP(18);
  asm volatile("s_waitcnt vmcnt(3)" : "+v"(pf), "+v"(b1v) :: "memory");
  if (!__all((((u64)pf[1] << 32) | pf[0]) == tag)) poll_flags(ws, kvf_off, tag);
  auto tail = [&](auto CW) __attribute__((always_inline)) {
  constexpr bool C = decltype(CW)::value;
  u32x4 uld; u32x2 gs, gc;
  asm volatile("buffer_load_dwordx4 %0, %1, %2, %3 offen sc1" : "=v"(uld) : "v"(lane * 16), "s"(sWS), "s"(WS_KVT + (ch0 + tg) * 4096 + fg * 1024) : "memory");
  BAR();
  STAMP(8);
  u32x4 wf[32];
  const unsigned w2so = WS_W2T + (nt * 112 + 16 * w) * 2048;
  auto ldw2 = [&](int n0, int n1) {
    if constexpr (C) {
#pragma unroll
      for (int n = n0; n < n1; ++n) wf[n] = __builtin_amdgcn_raw_buffer_load_b128(rWS, lane * 16 + (n & 3) * 1024, w2so + (n >> 2) * 4096, 16);
    }
    SB();
  };
  const unsigned gs_off = WS_STG + (128 * mt128 + 64 * tg + lane) * 8, gc_off = WS_CSG + (128 * ft + 32 * fg + r) * 8;
  STEP(19); ldw2(0, 1);
  STEP(20); ldw2(1, 2);
  STEP(21); ldw2(2, 3);
  STEP(22); ldw2(3, 4);
  if constexpr (!C) VMWAIT(0);
  asm volatile("s_waitcnt vmcnt(4)" : "+v"(uld) :: "memory");
  asm volatile("buffer_load_dwordx2 %0, %1, %2, 0 offen sc1" : "=v"(gs) : "v"(gs_off), "s"(sWS) : "memory");
  asm volatile("buffer_load_dwordx2 %0, %1, %2, 0 offen sc1" : "=v"(gc) : "v"(gc_off), "s"(sWS) : "memory");
  {
    const int n = 64 * fg + lane;
    sts128(L_U + tg * U_SZ + (n & 3) * U_PL + ((n >> 2) + 32) * 16, uld);
  }
  BAR();
  STAMP(9);
  STEP(23); ldw2(4, 5);
  STEP(24); ldw2(5, 6);
  STEP(25); ldw2(6, 7);
  STEP(26); ldw2(7, 8);
  STEP(27);
  if constexpr (!C) VMWAIT(0);
  asm volatile("s_waitcnt vmcnt(4)" : "+v"(gs), "+v"(gc) :: "memory");
  if (!__all(gs[1] == (unsigned)tag && gc[1] == (unsigned)tag)) { gs[0] = poll_gran(ws, gs_off, (unsigned)tag); gc[0] = poll_gran(ws, gc_off, (unsigned)tag); }
  STAMP(10);
  {
    if (fg == 0) *(LAS unsigned*)(L_ST + (64 * tg + lane) * 4) = gs[0];
    const unsigned nm = gs[0] ^ 0x8000u;
    const unsigned bm = __builtin_amdgcn_perm(nm, nm, 0x01000100u);
    const float c = __uint_as_float(gc[0]), chi = rbf(c), c1 = c - chi, cmid = rbf(c1);
    const u32x4 A = {pk(chi, cmid), pk(c1 - cmid, __uint_as_float(b1v)), 0u, 0u};
    acc0 = mfma(A, u32x4{hh ? 0u : bm, hh ? 0u : nm, 0u, 0u}, acc0);
    acc1 = mfma(A, u32x4{hh ? bm : 0u, hh ? nm : 0u, 0u, 0u}, acc1);
    const unsigned so = WS_HT + ((4 * mt128 + 2 * tg) * 112 + 8 * ft + 2 * fg) * 1024;
#pragma unroll
    for (int q = 0; q < 4; ++q) {
      u32x4 hv;
#pragma unroll
      for (int e = 0; e < 4; ++e) {
        float a0 = (q < 2 ? acc0 : acc1)[8 * (q & 1) + 2 * e], a1 = (q < 2 ? acc0 : acc1)[8 * (q & 1) + 2 * e + 1];
        a0 *= __builtin_fabsf(a0); OPAQUE(a0);
        a1 *= __builtin_fabsf(a1);
        const s16x2 m = __builtin_elementwise_max(__builtin_bit_cast(s16x2, pk(a0, a1)), s16x2{0, 0});
        hv[e] = __builtin_bit_cast(unsigned, m);
      }
      st128_sc1(hv, rWS, lane * 16 + (q & 1) * 1024, so + (q >> 1) * 112 * 1024);
    }
  }
  STAMP(11);
  const int tok = threadIdx.x >> 4, quad = threadIdx.x & 15;
  const unsigned hf_off = WS_HF + (mt64 * 56 + 8 * w + (lane & 7)) * FSTR;
  u32x2 hf, xr0, xr1, b2r;
  {
    f32x16 c0, c1;
#pragma unroll
    for (int i = 0; i < 16; ++i) { c0[i] = 0.f; c1[i] = 0.f; }
    unsigned ea0 = L_E + tg * E_SZ + 8 * (32 + r - 8 * hh - 7 + 256 * fg - 16), ea1 = ea0 + 32, ea2 = ea0 + 8192, ea3 = ea0 + 8224;
    unsigned ua = L_U + tg * U_SZ + hh * U_PL + 16 * (r + 32 - 8 * fg - 7);
    OPAQUE(ea0); OPAQUE(ea1); OPAQUE(ea2); OPAQUE(ea3); OPAQUE(ua);
    u32x4 A0[2], A1[2], B0[2], B1[2];
    auto ldc = [&](int st) {
      const int dd = st >> 1, s = st & 1;
      const unsigned eo = 8 * (32 * dd + 16 * (1 - s)), uo = 2 * s * U_PL + 16 * (7 - dd);
      u32x2 x0 = lds64(ea0 + eo), x1 = lds64(ea1 + eo);
      B0[st & 1] = lds128(ua + uo); B1[st & 1] = lds128(ua + uo + 512);
      u32x2 y0 = lds64(ea2 + eo), y1 = lds64(ea3 + eo);
      A0[st & 1] = u32x4{x0[0], x0[1], x1[0], x1[1]}; A1[st & 1] = u32x4{y0[0], y0[1], y1[0], y1[1]};
    };
    ldc(0);
    SB();
#pragma unroll
    for (int st = 0; st < 16; ++st) {
      if (st + 1 < 16) ldc(st + 1);
      SB();
      c0 = mfma(A0[st & 1], B0[st & 1], c0);
      c1 = mfma(A0[st & 1], B1[st & 1], c1);
      c1 = mfma(A1[st & 1], B0[st & 1], c1);
      asm volatile("" : "+v"(c0), "+v"(c1));
      SB();
      if (st == SIGF) {
        if constexpr (!C) VMWAIT(0);
        VMWAIT(2 * (SIGF < 12 ? SIGF : 12));
        flag_store(ws, WS_HF + ((2 * mt128 + tg) * 56 + 4 * ft + fg) * FSTR, tag, lane);
      }
      if (st == 10) __builtin_amdgcn_s_barrier();
      if (st < 12) ldw2(8 + 2 * st, 10 + 2 * st);
      if (st == 14) {
        const unsigned xo = ((64 * mt64 + tok) * D_ + 64 * nt + 4 * quad) * 2;
        asm volatile("buffer_load_dwordx2 %0, %1, %2, 0 offen" : "=v"(xr0) : "v"(xo), "s"(sX) : "memory");
        asm volatile("buffer_load_dwordx2 %0, %1, %2, %3 offen" : "=v"(xr1) : "v"(xo), "s"(sX), "s"(32 * 896) : "memory");
        asm volatile("global_load_dwordx2 %0, %1, %2" : "=v"(b2r) : "v"((64 * nt + 4 * quad) * 2), "s"(gb2) : "memory");
        asm volatile("buffer_load_dwordx2 %0, %1, %2, 0 offen sc1" : "=v"(hf) : "v"(hf_off), "s"(sWS) : "memory");
      }
    }
    STAMP(12);
    const unsigned pb = L_CP + w * P_SZ + lane * 16;
#pragma unroll
    for (int g4 = 0; g4 < 4; ++g4) {
      *(LAS f32x4*)(pb + g4 * 1088) = f32x4{c0[4 * g4], c0[4 * g4 + 1], c0[4 * g4 + 2], c0[4 * g4 + 3]};
      *(LAS f32x4*)(pb + 4352 + g4 * 1088) = f32x4{c1[4 * g4], c1[4 * g4 + 1], c1[4 * g4 + 2], c1[4 * g4 + 3]};
    }
  }
  asm volatile("s_waitcnt vmcnt(0)" : "+v"(hf), "+v"(xr0), "+v"(xr1), "+v"(b2r) :: "memory");
  constexpr int WIN = HWIN, NH0 = HFIRST;
  u32x4 hb[WIN];
  const unsigned hso = WS_HT + (2 * mt64 * 112 + 16 * w) * 1024;
  auto ldh = [&](int n) { return __builtin_amdgcn_raw_buffer_load_b128(rWS, lane * 16 + (n & 3) * 1024, hso + (n >> 4) * 112 * 1024 + (n & 12) * 1024, 16); };
  if constexpr (C) {
    if (!__all((((u64)hf[1] << 32) | hf[0]) == tag)) poll_flags(ws, hf_off, tag);
#pragma unroll
    for (int n = 0; n < NH0; ++n) hb[n] = ldh(n);
  }
  BAR();
  STAMP(13);
  {
    const int n = 32 * w + r, a = n >> 2, g4 = n & 3;
    const unsigned po = L_CP + hh * 4 * P_SZ + (a >> 5) * 4352 + g4 * 1088 + (a & 31) * 16;
    f32x4 lo = *(const LAS f32x4*)po, hi = *(const LAS f32x4*)(po + 512);
#pragma unroll
    for (int q = 1; q < 4; ++q) {
      lo += *(const LAS f32x4*)(po + q * P_SZ);
      hi += *(const LAS f32x4*)(po + q * P_SZ + 512);
    }
    const u32x4 kvc = lds128(L_U + hh * U_SZ + g4 * U_PL + (a + 32) * 16);
    const unsigned dlo = hh ? dpair >> 16 : dpair & 0xffffu, dhi = dlo << 16;
    float pd[8], yv[8];
    { const unsigned kk[8] = {kvc[3], kvc[2], kvc[1], kvc[0], kvc[3], kvc[2], kvc[1], kvc[0]}; dot2x8(pd, kk, dhi, dhi, dlo, dlo); }
    { const unsigned pp[8] = {pk(lo[0], pd[0]), pk(lo[1], pd[4]), pk(lo[2], pd[1]), pk(lo[3], pd[5]), pk(hi[0], pd[2]), pk(hi[1], pd[6]), pk(hi[2], pd[3]), pk(hi[3], pd[7])};
      dot2x8(yv, pp, ONES, ONES, ONES, ONES); }
    const u32x4 yo = {pk(yv[0], yv[1]), pk(yv[2], yv[3]), pk(yv[4], yv[5]), pk(yv[6], yv[7])};
    st128_sc1(yo, rWS, (ch0 + hh) * 4096 + n * 16, WS_YT);
  }
  STAMP(14);
  const unsigned yflag = WS_YF + ((cg * 8 + w) * 32 + tt) * FSTR;
  const unsigned rpo = quad * G_H + tok * 16;
  const unsigned st0 = *(const LAS unsigned*)(L_ST + (64 * hsel + tok) * 4), st1 = *(const LAS unsigned*)(L_ST + (64 * hsel + 32 + tok) * 4);
  auto gate = [&](unsigned base, float r2, unsigned (&gt)[2]) {
    unsigned ra = base + rpo;
    OPAQUE(ra);
    f32x4 sm = *(const LAS f32x4*)ra;
#pragma unroll
    for (int q = 1; q < 7; ++q) sm += *(const LAS f32x4*)(ra + q * P_SZ);
    const unsigned oa = pk(fmaf(sm[0], r2, bflo(b2r[0])), fmaf(sm[1], r2, bfhi(b2r[0]))), ob = pk(fmaf(sm[2], r2, bflo(b2r[1])), fmaf(sm[3], r2, bfhi(b2r[1])));
    const float o[4] = {bflo(oa), bfhi(oa), bflo(ob), bfhi(ob)};
    float sv[4];
#pragma unroll
    for (int e = 0; e < 4; ++e) sv[e] = o[e] * __builtin_amdgcn_rcpf(1.f + __builtin_amdgcn_exp2f(o[e] * -1.4426950408889634f));
    gt[0] = pk(sv[0], sv[1]); gt[1] = pk(sv[2], sv[3]);
  };
  auto xnorm = [&](u32x2 xr, unsigned st, unsigned (&xn)[2], float& r2) {
    const float m = bflo(st), rd = __builtin_amdgcn_rcpf(bfhi(st));
    const unsigned da = pk(bflo(xr[0]) - m, bfhi(xr[0]) - m), db = pk(bflo(xr[1]) - m, bfhi(xr[1]) - m);
    xn[0] = pk(bflo(da) * rd, bfhi(da) * rd); xn[1] = pk(bflo(db) * rd, bfhi(db) * rd);
    r2 = rd * rd;
  };
  unsigned g0[2], g1[2], xn0[2], xn1[2];
  float r20, r21;
  if constexpr (C) {
#pragma unroll
    for (int n = NH0; n < WIN; ++n) hb[n] = ldh(n);
    SB();
    STAMP(15);
    f32x16 ce, co;
#pragma unroll
    for (int mt = 0; mt < 2; ++mt) {
#pragma unroll
      for (int i = 0; i < 16; ++i) { ce[i] = 0.f; co[i] = 0.f; }
#pragma unroll
      for (int ks = 0; ks < 16; ++ks) {
        const int n = 16 * mt + ks;
        if (n == NH0) { VMWAIT(WIN); flag_store(ws, yflag, tag, lane); }
        ce = mfma(wf[2 * ks], hb[n % WIN], ce);
        co = mfma(wf[2 * ks + 1], hb[n % WIN], co);
        asm volatile("" : "+v"(ce), "+v"(co));
        if (n + WIN < 32) hb[n % WIN] = ldh(n + WIN);
        SB();
      }
      unsigned pb = (mt ? L_G1 : L_G0) + w * P_SZ + hh * G_H + r * 16;
      OPAQUE(pb);
#pragma unroll
      for (int g4 = 0; g4 < 4; ++g4) {
        *(LAS f32x4*)(pb + g4 * G_G) = f32x4{ce[4 * g4], ce[4 * g4 + 1], ce[4 * g4 + 2], ce[4 * g4 + 3]};
        *(LAS f32x4*)(pb + G_T + g4 * G_G) = f32x4{co[4 * g4], co[4 * g4 + 1], co[4 * g4 + 2], co[4 * g4 + 3]};
      }
      if (mt == 0) { BAR(); STAMP(16); xnorm(xr0, st0, xn0, r20); xnorm(xr1, st1, xn1, r21); gate(L_G0, r20, g0);
        asm volatile("" : "+v"(g0[0]), "+v"(g0[1]), "+v"(xn0[0]), "+v"(xn0[1]), "+v"(xn1[0]), "+v"(xn1[1]), "+v"(r21)); SB(); }
    }
    STAMP(17);
  } else {
    VMWAIT(0);
    flag_store(ws, yflag, tag, lane);
    xnorm(xr0, st0, xn0, r20); xnorm(xr1, st1, xn1, r21);
    STAMP(15);
    BAR();
    STAMP(16);
    gate(L_G0, r20, g0);
    poll_flags(ws, WS_YF + ((nt * 8 + (mt64 >> 2)) * 32 + r) * FSTR, tag);
    STAMP(17);
    u32x4 yb[8];
#pragma unroll
    for (int j = 0; j < 8; ++j) yb[j] = __builtin_amdgcn_raw_buffer_load_b128(rWS, (lane >> 3) * 4096 + (lane & 7) * 16, WS_YT + (64 * nt + 8 * j) * 4096 + mt64 * 128, 16);
#pragma unroll
    for (int j = 0; j < 8; ++j) {
      const unsigned ya = L_Y + (8 * j + (lane >> 3)) * 132 + (lane & 7) * 16;
#pragma unroll
      for (int e = 0; e < 4; ++e) *(LAS unsigned*)(ya + 4 * e) = yb[j][e];
    }
  }
  BAR();
  STAMP(18);
  {
    unsigned ya = L_Y + 4 * quad * 132 + tok * 2;
    OPAQUE(ya);
    unsigned yv[2][4];
#pragma unroll
    for (int mt = 0; mt < 2; ++mt)
#pragma unroll
      for (int e = 0; e < 4; ++e) yv[mt][e] = *(const LAS u16*)(ya + e * 132 + mt * 64);
    gate(L_G1, r21, g1);
#pragma unroll
    for (int mt = 0; mt < 2; ++mt) {
      const u32x2 xr = mt ? xr1 : xr0;
      const unsigned* xn = mt ? xn1 : xn0;
      const unsigned* gg = mt ? g1 : g0;
      const unsigned sa = pk(dot2(xn[0], yv[mt][0], 0.f), dot2(xn[0], yv[mt][1] << 16, 0.f)), sb = pk(dot2(xn[1], yv[mt][2], 0.f), dot2(xn[1], yv[mt][3] << 16, 0.f));
      const unsigned ma = pk(dot2(sa & 0xffffu, gg[0], 0.f), dot2(sa & 0xffff0000u, gg[0], 0.f));
      const unsigned mb = pk(dot2(sb & 0xffffu, gg[1], 0.f), dot2(sb & 0xffff0000u, gg[1], 0.f));
      const u32x2 o = {pk(bflo(ma) + bflo(xr[0]), bfhi(ma) + bfhi(xr[0])), pk(bflo(mb) + bflo(xr[1]), bfhi(mb) + bfhi(xr[1]))};
      *(GAS u32x2*)(gout + (64 * mt64 + 32 * mt + tok) * D_ + 64 * nt + 4 * quad) = o;
    }
  }
  };
  if (cw) tail(std::true_type{}); else tail(std::false_type{});
}

extern "C" void kernel_launch(void* const* d_in, const int* in_sizes, int n_in, void* d_out, int out_size, void* d_ws, size_t ws_size,
                              hipStream_t stream) {
  (void)in_sizes; (void)n_in; (void)out_size; (void)ws_size;
  (void)hipFuncSetAttribute((const void*)fused_h3_mlp, hipFuncAttributeMaxDynamicSharedMemorySize, LDS_BYTES);
  fused_h3_mlp<<<dim3(8, 8, 4), 512, LDS_BYTES, stream>>>((const u16*)d_in[0], (const u16*)d_in[1], (const u16*)d_in[2], (const u16*)d_in[3],
                                                         (const u16*)d_in[4], (const u16*)d_in[5], (const u16*)d_in[7], (const u16*)d_in[8],
                                                         (const u16*)d_in[9], (const u16*)d_in[10], (u16*)d_out, (char*)d_ws);
}
```

```cpp
#include <hip/hip_runtime.h>
#include <hip/hip_bf16.h>
#include <stdint.h>
#include <type_traits>

typedef unsigned short u16;
typedef unsigned long long u64;
typedef __bf16 bf16x8 __attribute__((ext_vector_type(8)));
typedef __bf16 bf16x2 __attribute__((ext_vector_type(2)));
typedef short s16x2 __attribute__((ext_vector_type(2)));
typedef short s16x4 __attribute__((ext_vector_type(4)));
typedef float f32x2 __attribute__((ext_vector_type(2)));
typedef float f32x4 __attribute__((ext_vector_type(4)));
typedef float f32x16 __attribute__((ext_vector_type(16)));
typedef unsigned u32x2 __attribute__((ext_vector_type(2)));
typedef unsigned u32x4 __attribute__((ext_vector_type(4)));
typedef int i32x4 __attribute__((ext_vector_type(4)));
#define GAS __attribute__((address_space(1)))
#define LAS __attribute__((address_space(3)))
#define DI __device__ __forceinline__
#pragma clang diagnostic ignored "-Wint-to-pointer-cast"

constexpr int S_ = 2048, D_ = 448, F_ = 1792;
constexpr int NWG = 224;
constexpr unsigned FSTR = 256;
constexpr unsigned WS_EPOCH = 0;
constexpr unsigned WS_KVF = 4096;
constexpr unsigned WS_HF = WS_KVF + 1792 * FSTR;
constexpr unsigned WS_YF = WS_HF + 1792 * FSTR;
constexpr unsigned WS_W2F = WS_YF + 1792 * FSTR;
constexpr unsigned WS_STG = WS_W2F + 1792 * FSTR;
constexpr unsigned WS_CSG = WS_STG + 2048 * 8;
constexpr unsigned WS_KVT = 2u << 20;
constexpr unsigned WS_YT = 4u << 20;
constexpr unsigned WS_HT = 6u << 20;
constexpr unsigned WS_W2T = WS_HT + 64u * 112u * 1024u;
constexpr unsigned WS_END = WS_W2T + 7u * 112u * 2048u;
static_assert(WS_CSG + 1792 * 8 <= WS_KVT, "ws");
constexpr unsigned WS_DBG = 16u << 20;
constexpr unsigned L_X = 49152;
constexpr unsigned L_W = 0;
constexpr unsigned L_E = 98304;
constexpr unsigned E_SZ = 16640;
constexpr unsigned U_PL = 1568, U_SZ = 4 * U_PL;
constexpr unsigned L_SH = L_E + 2 * E_SZ;
constexpr unsigned L_SS = L_SH + 8192 + 64;
constexpr unsigned L_KV = L_SS + 1024;
constexpr unsigned L_Y = L_KV;
constexpr unsigned L_ST = L_KV + 9216;
constexpr unsigned L_U = L_ST + 1024;
constexpr unsigned P_SZ = 8704;
constexpr unsigned L_CP = 0;
constexpr unsigned L_G0 = 69632;
constexpr unsigned G_SZ = 8448;
constexpr unsigned L_G1 = 0;
constexpr unsigned L_SK = L_U;
constexpr unsigned L_TP = L_U + 9216;
constexpr unsigned L_SV = L_KV;
constexpr unsigned L_KO = L_KV;
constexpr unsigned L_SW = L_SH;
constexpr unsigned G_H = 528, G_G = 1056, G_T = 4224;
constexpr unsigned LDS_BYTES = L_U + 2 * U_SZ;
static_assert(L_G0 + 8 * G_SZ <= L_KV && L_U % 16 == 0, "lds");
static_assert(LDS_BYTES <= 163840, "lds");

constexpr unsigned SPIN_CAP = 1u << 18;
constexpr unsigned ONES = 0x3f803f80u, ONE_HI = 0x3f800000u;

DI float bflo(unsigned v) { return __uint_as_float(v << 16); }
DI float bfhi(unsigned v) { return __uint_as_float(v & 0xffff0000u); }
DI unsigned pk(float lo, float hi) { f32x2 f = {lo, hi}; return __builtin_bit_cast(unsigned, __builtin_convertvector(f, bf16x2)); }
DI float fmul1(float a, float b) { float d; asm("v_mul_f32 %0, %1, %2" : "=v"(d) : "v"(a), "v"(b)); return d; }
DI float rbf(float a) { return bfhi(pk(a, a)); }
DI float dot2(unsigned a, unsigned b, float c) { return __builtin_amdgcn_fdot2_f32_bf16(__builtin_bit_cast(bf16x2, a), __builtin_bit_cast(bf16x2, b), c, false); }
DI void dot2x8(float (&d)[8], const unsigned (&a)[8], unsigned b0, unsigned b1, unsigned b2, unsigned b3) {
  asm volatile("v_dot2_f32_bf16 %0, %8, %16, 0\n\tv_dot2_f32_bf16 %1, %9, %16, 0\n\tv_dot2_f32_bf16 %2, %10, %17, 0\n\tv_dot2_f32_bf16 %3, %11, %17, 0\n\t"
               "v_dot2_f32_bf16 %4, %12, %18, 0\n\tv_dot2_f32_bf16 %5, %13, %18, 0\n\tv_dot2_f32_bf16 %6, %14, %19, 0\n\tv_dot2_f32_bf16 %7, %15, %19, 0\n\ts_nop 2"
               : "=&v"(d[0]), "=&v"(d[1]), "=&v"(d[2]), "=&v"(d[3]), "=&v"(d[4]), "=&v"(d[5]), "=&v"(d[6]), "=&v"(d[7])
               : "v"(a[0]), "v"(a[1]), "v"(a[2]), "v"(a[3]), "v"(a[4]), "v"(a[5]), "v"(a[6]), "v"(a[7]), "v"(b0), "v"(b1), "v"(b2), "v"(b3));
}
DI f32x16 mfma(u32x4 a, u32x4 b, f32x16 c) { return __builtin_amdgcn_mfma_f32_32x32x16_bf16(__builtin_bit_cast(bf16x8, a), __builtin_bit_cast(bf16x8, b), c, 0, 0, 0); }
DI u32x4 lds128(unsigned a) { return *(const LAS u32x4*)a; }
DI u32x2 lds64(unsigned a) { return *(const LAS u32x2*)a; }
DI u32x2 ldstr(unsigned a) { return __builtin_bit_cast(u32x2, __builtin_amdgcn_ds_read_tr16_b64_v4i16((LAS s16x4*)a)); }
DI void sts128(unsigned a, u32x4 v) { *(LAS u32x4*)a = v; }
DI i32x4 mksrd(const void* p, unsigned bytes) {
  u64 a = (u64)p;
  i32x4 r = {(int)(unsigned)a, (int)((unsigned)(a >> 32) & 0xffffu), (int)bytes, 0x00020000};
  return r;
}
#define VMWAIT(n) asm volatile("s_waitcnt vmcnt(%0)" ::"i"(n) : "memory")
#define BAR() asm volatile("s_waitcnt lgkmcnt(0)\n\ts_barrier" ::: "memory")
#define OPAQUE(x) asm volatile("" : "+v"(x))
#define SB() __builtin_amdgcn_sched_barrier(0)
#define ROR(x, n) __uint_as_float(__builtin_amdgcn_update_dpp(0u, __float_as_uint(x), 0x120 + (n), 0xf, 0xf, false))
#define PIN8(a) asm volatile("" : "+v"(a[0]), "+v"(a[1]), "+v"(a[2]), "+v"(a[3]), "+v"(a[4]), "+v"(a[5]), "+v"(a[6]), "+v"(a[7]))
template <int LOFF, int GOFF> DI void dma(unsigned wb, unsigned voff, i32x4 srd, unsigned soff) {
  asm volatile("s_add_i32 m0, %0, %4\n\ts_nop 0\n\tbuffer_load_dwordx4 %1, %2, %3 offen offset:%5 lds"
               ::"s"(wb), "v"(voff), "s"(srd), "s"(soff), "i"(LOFF - GOFF), "i"(GOFF) : "scc");
}
template <typename R> DI void st128_sc1(u32x4 v, R rsrc, unsigned voff, unsigned soff) {
  __builtin_amdgcn_raw_buffer_store_b128(v, rsrc, voff, soff, 16);
  asm volatile("s_nop 1" ::"v"(v) : "memory");
}
DI void flag_store(char* ws, unsigned off, u64 tag, int lane) {
  if (lane == 0) __hip_atomic_store((GAS u64*)(ws + off), tag, __ATOMIC_RELAXED, __HIP_MEMORY_SCOPE_AGENT);
}
DI void poll_flags(char* ws, unsigned off, u64 tag) {
#pragma unroll 1
  for (unsigned spins = 0; spins < SPIN_CAP; ++spins) {
    u64 f = __hip_atomic_load((GAS u64*)(ws + off), __ATOMIC_RELAXED, __HIP_MEMORY_SCOPE_AGENT);
    if (__all(f == tag)) break;
    __builtin_amdgcn_s_sleep(1);
  }
  asm volatile("" ::: "memory");
}

DI unsigned poll_gran(char* ws, unsigned off, unsigned tag) {
  u64 f = 0;
#pragma unroll 1
  for (unsigned spins = 0; spins < SPIN_CAP; ++spins) {
    f = __hip_atomic_load((GAS u64*)(ws + off), __ATOMIC_RELAXED, __HIP_MEMORY_SCOPE_AGENT);
    if (__all((unsigned)(f >> 32) == tag)) break;
    __builtin_amdgcn_s_sleep(1);
  }
  asm volatile("" ::: "memory");
  return (unsigned)f;
}

#define STAMP(i) do {} while (0)
#define PART42 1
#define SIGF 9
#define HF0 0
#define NOW2 0
#define KSW 5
#define HPOLL 12
#define HCHK 14
#define HPS 4
#define TAUX 16
#define HAUX 16
#define ABL 0
#define YPOLL 17
#define YLAG 6
#define OUTAUX 0
#define HFIRST 6
#define HWIN 12

__global__ __launch_bounds__(512, 2) void fused_h3_mlp(const u16* __restrict__ gx, const u16* __restrict__ gk, const u16* __restrict__ gv,
                                                      const u16* __restrict__ gshk, const u16* __restrict__ gssk, const u16* __restrict__ gsD,
                                                      const u16* __restrict__ gw1, const u16* __restrict__ gb1, const u16* __restrict__ gw2,
                                                      const u16* __restrict__ gb2, u16* __restrict__ gout, char* __restrict__ ws) {
  STAMP(0);
  const int g = blockIdx.x, yy = blockIdx.y, zz = blockIdx.z;
  const int w = __builtin_amdgcn_readfirstlane(threadIdx.x >> 6);
  const int lane = threadIdx.x & 63, r = lane & 31, hh = lane >> 5;
  const i32x4 sWS = mksrd(ws, WS_END);
  asm volatile("" ::"s"(gx), "s"(gk), "s"(gv), "s"(gshk), "s"(gssk), "s"(gsD), "s"(gw1), "s"(gb1), "s"(gw2), "s"(gb2), "s"(gout), "s"(ws));
  u64 tag;
  if (yy == 7) {
    const int a = g + 8 * zz;
    const i32x4 sX = mksrd(gx, S_ * D_ * 2), sW1 = mksrd(gw1, D_ * F_ * 2);
    u32x4 xr[8], wv[7];
    const int row0 = 256 * g + 64 * zz + 8 * w;
    const unsigned xo = lane < 56 && !(ABL & 4) ? lane * 16 : 0x7fffffffu;
#pragma unroll
    for (int j = 0; j < 8; ++j)
      asm volatile("buffer_load_dwordx4 %0, %1, %2, %3 offen" : "=v"(xr[j]) : "v"(xo), "s"(sX), "s"((row0 + j) * 896) : "memory");
    const int ci = (g >> 1) + 4 * zz, cb = 14 * (g & 1) + ci;
    const unsigned wo = ci < 14 && !(ABL & 4) ? (lane >> 3) * 3584 + (lane & 7) * 16 + cb * 128 : 0x7fffffffu;
#pragma unroll
    for (int j = 0; j < 7; ++j)
      asm volatile("buffer_load_dwordx4 %0, %1, %2, %3 offen" : "=v"(wv[j]) : "v"(wo), "s"(sW1), "s"((56 * w + 8 * j) * 3584) : "memory");
    asm volatile("s_load_dwordx2 %0, %1, 0x0\n\ts_waitcnt lgkmcnt(0)" : "=s"(tag) : "s"(ws) : "memory");
    tag += 1ull;
    {
      float v[16];
#pragma unroll
      for (int j = 0; j < 8; ++j) {
        asm volatile("s_waitcnt vmcnt(%1)" : "+v"(xr[j]) : "i"(14 - j) : "memory");
        float s = 0.f, q = 0.f;
#pragma unroll
        for (int e = 0; e < 4; ++e) { s = dot2(xr[j][e], ONES, s); q = dot2(xr[j][e], xr[j][e], q); }
        v[j] = s; v[8 + j] = q;
        asm volatile("" : "+v"(v[j]), "+v"(v[8 + j]));
      }
#pragma unroll
      for (int j = 0; j < 8; ++j) { auto t = __builtin_amdgcn_permlane32_swap(__float_as_uint(v[j]), __float_as_uint(v[8 + j]), false, false); v[j] = __uint_as_float(t[0]) + __uint_as_float(t[1]); }
#pragma unroll
      for (int j = 0; j < 4; ++j) { auto t = __builtin_amdgcn_permlane16_swap(__float_as_uint(v[j]), __float_as_uint(v[4 + j]), false, false); v[j] = __uint_as_float(t[0]) + __uint_as_float(t[1]); }
#pragma unroll
      for (int j = 0; j < 4; ++j) { v[j] += ROR(v[j], 8); v[j] += ROR(v[j], 4); v[j] += ROR(v[j], 2); v[j] += ROR(v[j], 1); }
      const float mine = (lane & 2) ? ((lane & 1) ? v[3] : v[2]) : ((lane & 1) ? v[1] : v[0]);
      const auto t = __builtin_amdgcn_permlane32_swap(__float_as_uint(mine), __float_as_uint(mine), false, false);
      const float sq = __uint_as_float(t[1]);
      const float mean = mine * (1.f / D_);
      const float var = fmaxf(sq * (1.f / D_) - mean * mean, 0.f);
      const float den = __builtin_amdgcn_sqrtf(rbf(rbf(var) + 1e-5f));
      if ((lane & 44) == 0)
        __hip_atomic_store((GAS u64*)(ws + WS_STG + (row0 + ((lane >> 4) & 1) * 4 + (lane & 3)) * 8), ((u64)(unsigned)tag << 32) | pk(mean, den), __ATOMIC_RELAXED, __HIP_MEMORY_SCOPE_AGENT);
    }
    {
      unsigned onelo = 0x3f80u; OPAQUE(onelo);
      float c[8];
#pragma unroll
      for (int e = 0; e < 8; ++e) c[e] = 0.f;
#pragma unroll
      for (int j = 0; j < 7; ++j) {
        asm volatile("s_waitcnt vmcnt(%1)" : "+v"(wv[j]) : "i"(7 - j) : "memory");
#pragma unroll
        for (int e = 0; e < 4; ++e) { c[2 * e] = dot2(wv[j][e], onelo, c[2 * e]); c[2 * e + 1] = dot2(wv[j][e], ONE_HI, c[2 * e + 1]); }
        PIN8(c);
      }
#pragma unroll
      for (int e = 0; e < 4; ++e) { auto t = __builtin_amdgcn_permlane32_swap(__float_as_uint(c[e]), __float_as_uint(c[4 + e]), false, false); c[e] = __uint_as_float(t[0]) + __uint_as_float(t[1]); }
#pragma unroll
      for (int e = 0; e < 2; ++e) { auto t = __builtin_amdgcn_permlane16_swap(__float_as_uint(c[e]), __float_as_uint(c[2 + e]), false, false); c[e] = __uint_as_float(t[0]) + __uint_as_float(t[1]); }
      c[0] += ROR(c[0], 8); c[1] += ROR(c[1], 8);
      *(LAS f32x2*)(w * 256 + (lane & 7) * 32 + (lane >> 4) * 8) = f32x2{c[0], c[1]};
      BAR();
      if (w == 0 && ci < 14) {
        float t = 0.f;
#pragma unroll
        for (int j = 0; j < 8; ++j) t += *(const LAS float*)(j * 256 + lane * 4);
        __hip_atomic_store((GAS u64*)(ws + WS_CSG + (64 * cb + lane) * 8), ((u64)(unsigned)tag << 32) | __float_as_uint(t), __ATOMIC_RELAXED, __HIP_MEMORY_SCOPE_AGENT);
      }
    }
    if (a == 31) {
#pragma unroll 1
      for (int j = w; j < 28; j += 8) poll_flags(ws, WS_HF + (j * 64 + lane) * FSTR, tag);
#pragma unroll 1
      for (int j = 0; j < 4; ++j) poll_gran(ws, WS_STG + ((w * 4 + j) * 64 + lane) * 8, (unsigned)tag);
      BAR();
      if (w == 0) flag_store(ws, WS_EPOCH, tag, lane);
    }
    return;
  }
  const int mt128 = 4 * (g >> 1) + zz, ft = 7 * (g & 1) + yy, hsel = g & 1;
  const int mt64 = 2 * mt128 + hsel, nt = yy;
  const int fg = w & 3, tg = w >> 2;
  const int cg = yy, tt = g + 8 * zz;
  const int ch0 = 64 * cg + 2 * tt;

  const i32x4 sX = mksrd(gx, S_ * D_ * 2), sW1 = mksrd(gw1, D_ * F_ * 2);
  unsigned vx0, vx1, vw0, vw1;
  {
    const int row = 16 * w + (lane >> 3);
    vx0 = (128 * mt128 + row) * 896 + (((lane ^ (lane >> 4)) & 7) << 4);
    vx1 = (vx0 + 8 * 896) ^ 64;
    const int q = lane >> 4;
    vw0 = (8 * w + q) * 3584 + ft * 256 + (((lane & 15) ^ ((q << 2) | (2 * (w & 1)))) << 4);
    vw1 = (vw0 + 4 * 3584) ^ 16;
  }
  const unsigned wb = w * 2048, wb1 = w * 1024;
#define SLX(k) ((k) == 3 ? L_E + 16384 : L_X + ((k) & 3) * 16384)
#define SLW(k) ((k) == 3 ? L_E : L_W + ((k) & 3) * 16384)
#define DMA1(k, j) do { \
    if ((j) == 0) dma<SLX(k), (k) * 128>(wb, vx0, sX, 0); \
    if ((j) == 1) dma<SLW(k), 0>(wb, vw0, sW1, (k) * 64 * 3584); \
    if ((j) == 2) dma<SLX(k) + 1024, (k) * 128>(wb, vx1, sX, 0); \
    if ((j) == 3) dma<SLW(k) + 1024, 0>(wb, vw1, sW1, (k) * 64 * 3584); } while (0)
  DMA1(0, 0); DMA1(0, 1); DMA1(0, 2); DMA1(0, 3);
  __builtin_amdgcn_s_barrier();
  const unsigned vr8 = (lane >> 3) * 896 + (lane & 7) * 16;
  {
    const unsigned kvso = 64 * tt * 896 + cg * 128;
    const i32x4 sK = mksrd(gk, S_ * D_ * 2);
    if (w == 4) dma<L_SK, 0>(0, tt ? vr8 : 0x7fffffffu, sK, tt ? kvso - 8 * 896 : 0);
    if (w == 5) dma<L_TP, 0>(0, lane * 16, mksrd(gshk, D_ * 8), cg * 512);
    dma<L_SK + 1024, 0>(wb1, vr8, sK, kvso + 8 * w * 896);
    dma<L_SV, 0>(wb1, vr8, mksrd(gv, S_ * D_ * 2), kvso + 8 * w * 896);
  }
  DMA1(1, 0); DMA1(1, 1); DMA1(1, 2); DMA1(1, 3);
  const auto rWS = __builtin_amdgcn_make_buffer_rsrc(ws, 0, WS_END, 0x00020000);
  unsigned dpair;
  asm volatile("s_load_dwordx2 %0, %2, 0x0\n\ts_load_dword %1, %3, 0x0" : "=&s"(tag), "=&s"(dpair) : "s"(ws), "s"(gsD + ch0) : "memory");

  unsigned aA0, aA1, aB[4];
  const int blk = (lane >> 4) & 1, q4 = (lane & 15) >> 2, p4 = lane & 3;
  {
    const int ch = 4 * fg + 2 * blk + (p4 >> 1);
    aA0 = L_W + 256 * (8 * hh + q4) + 16 * (ch ^ ((q4 << 2) | (2 * hh))) + 8 * (p4 & 1);
    aA1 = (aA0 + 1024) ^ 16;
    const unsigned b0 = L_X + (64 * tg + r) * 128 + ((hh ^ ((r >> 1) & 7)) << 4);
#pragma unroll
    for (int j = 0; j < 4; ++j) { aB[j] = b0 ^ (j << 5); OPAQUE(aB[j]); }
    OPAQUE(aA0); OPAQUE(aA1);
  }
  f32x16 acc0, acc1;
#pragma unroll
  for (int i = 0; i < 16; ++i) { acc0[i] = 0.f; acc1[i] = 0.f; }
  u32x4 fA[2], fB0[2], fB1[2];
  auto ldf = [&](int n) {
    const int j = n & 3, k = n >> 2;
    unsigned pa0 = aA0, pa1 = aA1, pb = aB[j];
    if (k == 3) { pa0 += L_E - L_W; pa1 += L_E - L_W; pb += L_E + 16384 - L_X; OPAQUE(pa0); OPAQUE(pa1); OPAQUE(pb); }
    const unsigned so = k == 3 ? 0 : (k & 3) * 16384;
    u32x2 a0 = ldstr(pa0 + so + j * 4096), a1 = ldstr(pa1 + so + j * 4096);
    fA[n & 1] = u32x4{a0[0], a0[1], a1[0], a1[1]};
    fB0[n & 1] = lds128(pb + so); fB1[n & 1] = lds128(pb + so + 4096);
  };
#define STEP(n) do { \
    if ((n) + 1 < 28) ldf((n) + 1); \
    SB(); \
    acc0 = mfma(fA[(n) & 1], fB0[(n) & 1], acc0); \
    acc1 = mfma(fA[(n) & 1], fB1[(n) & 1], acc1); \
    asm volatile("" : "+v"(acc0), "+v"(acc1)); \
    SB(); \
  } while (0)
#define PIECE(m) DMA1(((m) >> 2) + 2, (m) & 3)
  STAMP(1);
  asm volatile("s_waitcnt vmcnt(6) lgkmcnt(0)\n\ts_barrier" : "+s"(tag), "+s"(dpair) :: "memory");
  tag += 1ull;
  STAMP(2);
  ldf(0);
  dma<L_SW, 0>(wb1, vr8, mksrd(gw2, tt < 28 ? D_ * F_ * 2 : 0), (64 * tt + 8 * w) * 896 + cg * 128);
  STEP(0); PIECE(0); STEP(1); PIECE(1); STEP(2); PIECE(2);
  VMWAIT(3); BAR();
  STAMP(3);
  u32x4 w2u;
  {
    const unsigned ta = (8 * w + q4) * 128 + (lane >> 4) * 32 + p4 * 8;
    u32x2 k0 = ldstr(L_SK + 512 + ta), k1 = ldstr(L_SK + 1024 + ta), k2 = ldstr(L_SK + 1536 + ta);
    u32x2 v0 = ldstr(L_SV + ta), v1 = ldstr(L_SV + 512 + ta);
    u32x2 shk = lds64(L_TP + lane * 8);
    const unsigned wa = L_SW + (16 * (w >> 1) + 4 * hh + q4) * 128 + (w & 1) * 64 + blk * 32 + p4 * 8;
    const u32x2 wa0 = ldstr(wa), wa1 = ldstr(wa + 1024);
    w2u = u32x4{wa0[0], wa0[1], wa1[0], wa1[1]};
    STEP(3);
    PIECE(3);
    BAR();
    PIECE(4);
    const float kr[11] = {bfhi(k0[0]), bflo(k0[1]), bfhi(k0[1]), bflo(k1[0]), bfhi(k1[0]), bflo(k1[1]), bfhi(k1[1]), bflo(k2[0]), bfhi(k2[0]), bflo(k2[1]), bfhi(k2[1])};
    const float tw0 = bflo(shk[0]), tw1 = bfhi(shk[0]), tw2 = bflo(shk[1]), tw3 = bfhi(shk[1]);
    float a[8]; unsigned pp[8];
#pragma unroll
    for (int j = 0; j < 8; ++j) pp[j] = pk(fmul1(kr[j], tw3), fmul1(kr[j + 1], tw2));
    PIECE(5);
    dot2x8(a, pp, ONES, ONES, ONES, ONES);
#pragma unroll
    for (int j = 0; j < 8; ++j) pp[j] = pk(a[j], fmul1(kr[j + 2], tw1));
    PIECE(6);
    dot2x8(a, pp, ONES, ONES, ONES, ONES);
#pragma unroll
    for (int j = 0; j < 8; ++j) pp[j] = pk(a[j], fmul1(kr[j + 3], tw0));
    PIECE(7);
    dot2x8(a, pp, ONES, ONES, ONES, ONES);
#pragma unroll
    for (int j = 0; j < 8; j += 2) { pp[j] = pk(a[j], 0.f); pp[j + 1] = pk(0.f, a[j + 1]); }
    dot2x8(a, pp, v0[0], v0[1], v1[0], v1[1]);
    sts128(L_KO + lane * 128 + ((w ^ (lane & 7)) << 4), u32x4{pk(a[7], a[6]), pk(a[5], a[4]), pk(a[3], a[2]), pk(a[1], a[0])});
    SB();
  }
  STAMP(20);
  STEP(4); PIECE(8); STEP(5); PIECE(9); STEP(6); PIECE(10);
  VMWAIT(7); BAR();
  STAMP(4);
  {
    const u32x4 o = lds128(L_KO + (8 * w + (lane >> 3)) * 128 + (((lane ^ (lane >> 3)) & 7) << 4));
    st128_sc1(w2u, rWS, lane * 16, tt < 28 ? WS_W2T + (cg * 112 + 4 * tt) * 2048 + w * 1024 : WS_END);
    STEP(7);
    st128_sc1(o, rWS, (64 * cg + 8 * w + (lane >> 3)) * 4096 + (lane & 7) * 16, WS_KVT + tt * 128);
  }
  bool ksent = false;
#define TRYK(Y, FORCE) do { if (!ksent) { const unsigned ib = __builtin_amdgcn_s_getreg(7 | (31 << 11)); \
      if ((FORCE) || ((ib & 15u) | ((ib >> 18) & 48u)) <= (Y)) { VMWAIT(Y); flag_store(ws, WS_KVF + ((cg * 32 + tt) * 8 + w) * FSTR, tag, lane); ksent = true; } } } while (0)
  PIECE(11); STEP(8); PIECE(12); STEP(9); PIECE(13); STEP(10); PIECE(14);
  VMWAIT(9); TRYK(4, false); BAR();
  STAMP(5);
  STEP(11); PIECE(15); TRYK(5, false); STEP(12); PIECE(16); TRYK(6, false); STEP(13); PIECE(17); TRYK(7, false); STEP(14); PIECE(18);
  VMWAIT(7); TRYK(8, true); BAR();
  STAMP(6);
  STEP(15); PIECE(19);
  STEP(16); dma<L_SH, 0>(wb1, lane * 16, mksrd(gssk, S_ * D_ * 2), ch0 * 4096 + wb1);
  STEP(17);
  if (w < 4) sts128(L_U + (w >> 1) * U_SZ + ((w & 1) * 2 + hh) * U_PL + r * 16, u32x4{0u, 0u, 0u, 0u});
  const unsigned kvf_off = lane < 8 ? WS_KVF + (cg * 256 + (8 * fg + lane) * 8 + (tt >> 2)) * FSTR : WS_KVF + (cg * 256 + 28 * w + (lane < 36 ? lane - 8 : 0)) * FSTR;
  const unsigned gs_off = WS_STG + (128 * mt128 + 64 * tg + lane) * 8, gc_off = WS_CSG + (128 * ft + 32 * fg + r) * 8;
  u32x2 pf, gs, gc; unsigned b1v = 0; u32x4 uld;
  STEP(18);
  VMWAIT(5); BAR();
  STAMP(7);
  STEP(19);
  asm volatile("buffer_load_dwordx2 %0, %1, %2, 0 offen sc1" : "=v"(pf) : "v"(kvf_off), "s"(sWS) : "memory");
  asm volatile("global_load_short_d16_hi %0, %1, %2" : "+v"(b1v) : "v"((128 * ft + 32 * fg + r) * 2), "s"(gb1) : "memory");
  STEP(20); STEP(21); STEP(22);
  VMWAIT(2); BAR();
  STAMP(8);
  STEP(23);
  asm volatile("buffer_load_dwordx2 %0, %1, %2, 0 offen sc1" : "=v"(gs) : "v"(gs_off), "s"(sWS) : "memory");
  asm volatile("buffer_load_dwordx2 %0, %1, %2, 0 offen sc1" : "=v"(gc) : "v"(gc_off), "s"(sWS) : "memory");
  unsigned ed[4][3];
  {
    unsigned ra = L_SH + w * 1024 + lane * 4;
    OPAQUE(ra);
#pragma unroll
    for (int j = 0; j < 4; ++j)
#pragma unroll
      for (int e = 0; e < 3; ++e) ed[j][e] = *(const LAS unsigned*)(ra + j * 256 + e * 4);
  }
  STEP(24);
  asm volatile("s_waitcnt vmcnt(2)" : "+v"(pf), "+v"(b1v) :: "memory");
  if (!__all((((u64)pf[1] << 32) | pf[0]) == tag)) poll_flags(ws, kvf_off, tag);
  asm volatile("buffer_load_dwordx4 %0, %1, %2, %3 offen sc1" : "=v"(uld) : "v"(lane * 16), "s"(sWS), "s"(WS_KVT + (ch0 + tg) * 4096 + fg * 1024) : "memory");
  u32x4 wf[28];
  unsigned l16 = lane * 16; OPAQUE(l16);
  const unsigned w2so = WS_W2T + (nt * 112 + 14 * w) * 2048;
  auto ldw2 = [&](int n0, int n1) {
#pragma unroll
    for (int n = n0; n < n1; ++n) { wf[n] = __builtin_amdgcn_raw_buffer_load_b128(rWS, (ABL & 16) ? 0x7fffffffu : l16 + (n & 3) * 1024, w2so + (n >> 2) * 4096, TAUX); SB(); }
  };
  STEP(25); ldw2(0, 2);
  STEP(26); ldw2(2, 4);
  STEP(27); ldw2(4, 5);
  asm volatile("s_waitcnt vmcnt(6)" : "+v"(gs), "+v"(gc) :: "memory");
  if (!__all(gs[1] == (unsigned)tag && gc[1] == (unsigned)tag)) { gs[0] = poll_gran(ws, gs_off, (unsigned)tag); gc[0] = poll_gran(ws, gc_off, (unsigned)tag); }
  STAMP(10);
  {
    if (fg == 0) *(LAS unsigned*)(L_ST + (64 * tg + lane) * 4) = gs[0];
    const unsigned nm = gs[0] ^ 0x8000u;
    const unsigned bm = __builtin_amdgcn_perm(nm, nm, 0x01000100u);
    const float c = __uint_as_float(gc[0]), chi = rbf(c), c1 = c - chi, cmid = rbf(c1);
    const u32x4 A = {pk(chi, cmid), pk(c1 - cmid, __uint_as_float(b1v)), 0u, 0u};
    acc0 = mfma(A, u32x4{hh ? 0u : bm, hh ? 0u : nm, 0u, 0u}, acc0);
    acc1 = mfma(A, u32x4{hh ? bm : 0u, hh ? nm : 0u, 0u, 0u}, acc1);
    const unsigned so = WS_HT + ((4 * mt128 + 2 * tg) * 112 + 8 * ft + 2 * fg) * 1024;
    unsigned wa = L_E + 256 + tg * E_SZ + fg * 4096 + lane * 16;
    OPAQUE(wa);
#pragma unroll
    for (int q = 0; q < 4; ++q) {
      sts128(wa + q * 1024, u32x4{ed[q][0], ed[q][1], __builtin_amdgcn_alignbit(ed[q][1], ed[q][0], 16), __builtin_amdgcn_alignbit(ed[q][2], ed[q][1], 16)});
      u32x4 hv;
#pragma unroll
      for (int e = 0; e < 4; ++e) {
        float a0 = (q < 2 ? acc0 : acc1)[8 * (q & 1) + 2 * e], a1 = (q < 2 ? acc0 : acc1)[8 * (q & 1) + 2 * e + 1];
        a0 *= __builtin_fabsf(a0); OPAQUE(a0);
        a1 *= __builtin_fabsf(a1);
        const s16x2 m = __builtin_elementwise_max(__builtin_bit_cast(s16x2, pk(a0, a1)), s16x2{0, 0});
        hv[e] = __builtin_bit_cast(unsigned, m);
      }
      st128_sc1(hv, rWS, (ABL & 8) ? 0x7fffffffu : l16 + (q & 1) * 1024, so + (q >> 1) * 112 * 1024);
    }
  }
  STAMP(11);
  asm volatile("s_waitcnt vmcnt(9)" : "+v"(uld) :: "memory");
  {
    const int n = 64 * fg + lane;
    sts128(L_U + tg * U_SZ + (n & 3) * U_PL + ((n >> 2) + 32) * 16, uld);
    if (fg == 0) {
      const unsigned h0 = __builtin_amdgcn_readfirstlane(ed[0][0]), h1 = __builtin_amdgcn_readfirstlane(__builtin_amdgcn_alignbit(ed[0][1], ed[0][0], 16));
      const int l = lane & 15;
      sts128(L_E + tg * E_SZ + l * 16, u32x4{0u, l == 15 ? h0 : 0u, l == 15 ? h0 << 16 : 0u, l == 15 ? h1 : l == 14 ? h0 << 16 : 0u});
    }
  }
  ldw2(5, 7);
  BAR();
  STAMP(21);
  const int tok = threadIdx.x >> 4, quad = threadIdx.x & 15;
  const unsigned hf_off = WS_HF + (mt64 * 56 + 7 * w + (lane < 7 ? lane : 6)) * FSTR;
  const unsigned xo = ((64 * mt64 + tok) * D_ + 64 * nt + 4 * quad) * 2;
  u32x2 hf, xr0, xr1, b2r;
  constexpr int WIN = HWIN, NH0 = (16 - HCHK) * HPS;
  u32x4 hb[WIN];
  const unsigned hso = WS_HT + (2 * mt64 * 112 + 14 * w) * 1024;
  auto ldh = [&](int n) {
    const int mt = n / 14, ks = n % 14;
    hb[n % WIN] = __builtin_amdgcn_raw_buffer_load_b128(rWS, (ABL & 32) ? 0x7fffffffu : l16 + (ks & 3) * 1024, hso + mt * 112 * 1024 + (ks >> 2) * 4096, HAUX);
    SB();
  };
  {
    f32x16 c0, c1;
#pragma unroll
    for (int i = 0; i < 16; ++i) { c0[i] = 0.f; c1[i] = 0.f; }
    unsigned ea0 = L_E + tg * E_SZ + 8 * (32 + r - 8 * hh - 7 + 256 * fg - 16), ea1 = ea0 + 32, ea2 = ea0 + 8192, ea3 = ea0 + 8224;
    unsigned ua = L_U + tg * U_SZ + hh * U_PL + 16 * (r + 32 - 8 * fg - 7);
    OPAQUE(ea0); OPAQUE(ea1); OPAQUE(ea2); OPAQUE(ea3); OPAQUE(ua);
    u32x4 A0[2], A1[2], B0[2], B1[2];
    auto ldc = [&](int st) {
      const int dd = st >> 1, s = st & 1;
      const unsigned eo = 8 * (32 * dd + 16 * (1 - s)), uo = 2 * s * U_PL + 16 * (7 - dd);
      u32x2 x0 = lds64(ea0 + eo), x1 = lds64(ea1 + eo);
      B0[st & 1] = lds128(ua + uo); B1[st & 1] = lds128(ua + uo + 512);
      u32x2 y0 = lds64(ea2 + eo), y1 = lds64(ea3 + eo);
      A0[st & 1] = u32x4{x0[0], x0[1], x1[0], x1[1]}; A1[st & 1] = u32x4{y0[0], y0[1], y1[0], y1[1]};
    };
    ldc(0);
    SB();
    bool hsent = false;
#pragma unroll
    for (int st = 0; st < 16; ++st) {
      if (st + 1 < 16) ldc(st + 1);
      SB();
      c0 = mfma(A0[st & 1], B0[st & 1], c0);
      c1 = mfma(A0[st & 1], B1[st & 1], c1);
      c1 = mfma(A1[st & 1], B0[st & 1], c1);
      asm volatile("" : "+v"(c0), "+v"(c1));
      SB();
      if (st >= HF0 && st <= SIGF && !hsent) {
        const int younger = 2 + (st < 9 ? 2 * st : 18 + (st - 9));
        const unsigned ib = __builtin_amdgcn_s_getreg(7 | (31 << 11));
        if (st == SIGF || ((ib & 15u) | ((ib >> 18) & 48u)) <= (unsigned)younger) {
          VMWAIT(younger);
          flag_store(ws, WS_HF + ((2 * mt128 + tg) * 56 + 4 * ft + fg) * FSTR, tag, lane);
          hsent = true;
        }
      }
      if (st < 9) ldw2(7 + 2 * st, 9 + 2 * st); else if (st < 12) ldw2(16 + st, 17 + st);
      if (st == 12) {
        asm volatile("buffer_load_dwordx2 %0, %1, %2, 0 offen" : "=v"(xr0) : "v"(xo), "s"(sX) : "memory");
        asm volatile("buffer_load_dwordx2 %0, %1, %2, %3 offen" : "=v"(xr1) : "v"(xo), "s"(sX), "s"(32 * 896) : "memory");
        asm volatile("global_load_dwordx2 %0, %1, %2" : "=v"(b2r) : "v"((64 * nt + 4 * quad) * 2), "s"(gb2) : "memory");
      }
      if (st == HPOLL) asm volatile("buffer_load_dwordx2 %0, %1, %2, 0 offen sc1" : "=v"(hf) : "v"(hf_off), "s"(sWS) : "memory");
      if (st == HCHK) {
        STAMP(9);
        asm volatile("s_waitcnt vmcnt(0)" : "+v"(hf), "+v"(xr0), "+v"(xr1), "+v"(b2r) :: "memory");
        if (!__all((((u64)hf[1] << 32) | hf[0]) == tag)) poll_flags(ws, hf_off, tag);
        STAMP(22);
      }
      if (st >= HCHK) {
#pragma unroll
        for (int n = (st - HCHK) * HPS; n < (st - HCHK + 1) * HPS; ++n) ldh(n);
      }
    }
    STAMP(12);
    const unsigned pb = L_CP + w * P_SZ + lane * 16;
#pragma unroll
    for (int g4 = 0; g4 < 4; ++g4) {
      *(LAS f32x4*)(pb + g4 * 1088) = f32x4{c0[4 * g4], c0[4 * g4 + 1], c0[4 * g4 + 2], c0[4 * g4 + 3]};
      *(LAS f32x4*)(pb + 4352 + g4 * 1088) = f32x4{c1[4 * g4], c1[4 * g4 + 1], c1[4 * g4 + 2], c1[4 * g4 + 3]};
    }
  }
  BAR();
  STAMP(13);
  {
    const int n = 32 * w + r, a = n >> 2, g4 = n & 3;
    const unsigned po = L_CP + hh * 4 * P_SZ + (a >> 5) * 4352 + g4 * 1088 + (a & 31) * 16;
    f32x4 lo = *(const LAS f32x4*)po, hi = *(const LAS f32x4*)(po + 512);
#pragma unroll
    for (int q = 1; q < 4; ++q) {
      lo += *(const LAS f32x4*)(po + q * P_SZ);
      hi += *(const LAS f32x4*)(po + q * P_SZ + 512);
    }
    const u32x4 kvc = lds128(L_U + hh * U_SZ + g4 * U_PL + (a + 32) * 16);
    const unsigned dlo = hh ? dpair >> 16 : dpair & 0xffffu, dhi = dlo << 16;
    float pd[8], yv[8];
    { const unsigned kk[8] = {kvc[3], kvc[2], kvc[1], kvc[0], kvc[3], kvc[2], kvc[1], kvc[0]}; dot2x8(pd, kk, dhi, dhi, dlo, dlo); }
    { const unsigned pp[8] = {pk(lo[0], pd[0]), pk(lo[1], pd[4]), pk(lo[2], pd[1]), pk(lo[3], pd[5]), pk(hi[0], pd[2]), pk(hi[1], pd[6]), pk(hi[2], pd[3]), pk(hi[3], pd[7])};
      dot2x8(yv, pp, ONES, ONES, ONES, ONES); }
    const u32x4 yo = {pk(yv[0], yv[1]), pk(yv[2], yv[3]), pk(yv[4], yv[5]), pk(yv[6], yv[7])};
    st128_sc1(yo, rWS, (ch0 + hh) * 4096 + n * 16, WS_YT);
  }
  STAMP(14);
#pragma unroll
  for (int n = NH0; n < WIN; ++n) ldh(n);
  auto gate = [&](unsigned ra, float r2, unsigned (&gt)[2]) {
    f32x4 sm = *(const LAS f32x4*)ra;
#pragma unroll
    for (int q = 1; q < 8; ++q) sm += *(const LAS f32x4*)(ra + q * G_SZ);
    const unsigned oa = pk(fmaf(sm[0], r2, bflo(b2r[0])), fmaf(sm[1], r2, bfhi(b2r[0]))), ob = pk(fmaf(sm[2], r2, bflo(b2r[1])), fmaf(sm[3], r2, bfhi(b2r[1])));
    const float o[4] = {bflo(oa), bfhi(oa), bflo(ob), bfhi(ob)};
    float sv[4];
#pragma unroll
    for (int e = 0; e < 4; ++e) sv[e] = o[e] * __builtin_amdgcn_rcpf(1.f + __builtin_amdgcn_exp2f(o[e] * -1.4426950408889634f));
    gt[0] = pk(sv[0], sv[1]); gt[1] = pk(sv[2], sv[3]);
  };
  auto xnorm = [&](u32x2 xr, unsigned st, unsigned (&xn)[2], float& r2, float* xf) {
    const float m = bflo(st), rd = __builtin_amdgcn_rcpf(bfhi(st));
    xf[0] = bflo(xr[0]); xf[1] = bfhi(xr[0]); xf[2] = bflo(xr[1]); xf[3] = bfhi(xr[1]);
    const unsigned da = pk(xf[0] - m, xf[1] - m), db = pk(xf[2] - m, xf[3] - m);
    xn[0] = pk(bflo(da) * rd, bfhi(da) * rd); xn[1] = pk(bflo(db) * rd, bfhi(db) * rd);
    r2 = rd * rd;
  };
  unsigned g0[2], g1[2], xn0[2], xn1[2];
  float r20, r21, xf[8];
  unsigned rp0 = L_G0 + quad * G_H + tok * 16, rp1 = rp0 - (L_G0 - L_G1), yra = L_Y + 4 * quad * 132 + tok * 2;
  const unsigned yf_off = WS_YF + ((nt * 8 + (mt64 >> 2)) * 32 + 4 * w + (lane & 3)) * FSTR;
  u32x2 yf; u32x4 yb;
  const unsigned yvo = (lane >> 3) * 4096 + (lane & 7) * 16, yso = WS_YT + (64 * nt + 8 * w) * 4096 + mt64 * 128;
  STAMP(15);
  {
    f32x16 ca[2], cb[2];
#pragma unroll
    for (int i = 0; i < 16; ++i) { ca[0][i] = 0.f; cb[0][i] = 0.f; ca[1][i] = 0.f; cb[1][i] = 0.f; }
    unsigned pb0 = L_G0 + w * G_SZ + hh * G_H + r * 16, pb1 = pb0 - (L_G0 - L_G1);
    OPAQUE(pb0); OPAQUE(pb1);
    auto wrp = [&](unsigned pb, const f32x16& c, int g4, int t) { *(LAS f32x4*)(pb + t * G_T + g4 * G_G) = f32x4{c[4 * g4], c[4 * g4 + 1], c[4 * g4 + 2], c[4 * g4 + 3]}; };
#pragma unroll
    for (int n = 0; n < 28; ++n) {
      const int mt = n / 14, ks = n % 14;
      if (n == NH0) { VMWAIT(WIN); flag_store(ws, WS_YF + ((cg * 8 + w) * 32 + tt) * FSTR, tag, lane); }
      ca[mt] = mfma(wf[2 * ks], hb[n % WIN], ca[mt]);
      cb[mt] = mfma(wf[2 * ks + 1], hb[n % WIN], cb[mt]);
      asm volatile("" : "+v"(ca[mt]), "+v"(cb[mt]));
      SB();
      if (n + WIN < 28) ldh(n + WIN);
      if (n == 1) {
        const unsigned st0 = *(const LAS unsigned*)(L_ST + (64 * hsel + tok) * 4), st1 = *(const LAS unsigned*)(L_ST + (64 * hsel + 32 + tok) * 4);
        xnorm(xr0, st0, xn0, r20, xf); xnorm(xr1, st1, xn1, r21, xf + 4);
        asm volatile("" : "+v"(xn0[0]), "+v"(xn0[1]), "+v"(xn1[0]), "+v"(xn1[1]), "+v"(r20), "+v"(r21), "+v"(rp0), "+v"(rp1), "+v"(yra)); PIN8(xf); SB();
      }
      if (n >= 14 && n < 18) { wrp(pb0, ca[0], n - 14, 0); wrp(pb0, cb[0], n - 14, 1); SB(); }
      if (n == 20) { BAR(); STAMP(16); }
      if (n == YPOLL) asm volatile("buffer_load_dwordx2 %0, %1, %2, 0 offen sc1" : "=v"(yf) : "v"(yf_off), "s"(sWS) : "memory");
      if (n == YPOLL + YLAG) {
        asm volatile("s_waitcnt vmcnt(0)" : "+v"(yf) :: "memory");
        asm volatile("buffer_load_dwordx4 %0, %1, %2, %3 offen sc1" : "=v"(yb) : "v"(yvo), "s"(sWS), "s"(yso) : "memory");
      }
    }
#pragma unroll
    for (int g4 = 0; g4 < 4; ++g4) { wrp(pb1, ca[1], g4, 0); wrp(pb1, cb[1], g4, 1); }
    SB();
    gate(rp0, r20, g0);
  }
  STAMP(17);
  asm volatile("s_waitcnt vmcnt(0)" : "+v"(yb) :: "memory");
  if (!__all((((u64)yf[1] << 32) | yf[0]) == tag)) {
    poll_flags(ws, yf_off, tag);
    asm volatile("buffer_load_dwordx4 %0, %1, %2, %3 offen sc1\n\ts_waitcnt vmcnt(0)" : "=&v"(yb) : "v"(yvo), "s"(sWS), "s"(yso) : "memory");
  }
  {
    const unsigned ya = L_Y + (8 * w + (lane >> 3)) * 132 + (lane & 7) * 16;
#pragma unroll
    for (int e = 0; e < 4; ++e) *(LAS unsigned*)(ya + 4 * e) = yb[e];
  }
  BAR();
  STAMP(18);
  {
    unsigned yv[8];
#pragma unroll
    for (int e = 0; e < 8; ++e) yv[e] = *(const LAS u16*)(yra + (e & 3) * 132 + (e >> 2) * 64);
    gate(rp1, r21, g1);
    float t[8]; unsigned pp[8];
#pragma unroll
    for (int e = 1; e < 8; e += 2) yv[e] <<= 16;
    dot2x8(t, yv, xn0[0], xn0[1], xn1[0], xn1[1]);
#pragma unroll
    for (int e = 0; e < 8; e += 2) { pp[e] = pk(t[e], 0.f); pp[e + 1] = pk(0.f, t[e + 1]); }
    dot2x8(t, pp, g0[0], g0[1], g1[0], g1[1]);
#pragma unroll
    for (int e = 0; e < 8; ++e) pp[e] = pk(t[e], xf[e]);
    dot2x8(t, pp, ONES, ONES, ONES, ONES);
    const auto rO = __builtin_amdgcn_make_buffer_rsrc(gout, 0, S_ * D_ * 2, 0x00020000);
    __builtin_amdgcn_raw_buffer_store_b64(u32x2{pk(t[0], t[1]), pk(t[2], t[3])}, rO, xo, 0, OUTAUX);
    __builtin_amdgcn_raw_buffer_store_b64(u32x2{pk(t[4], t[5]), pk(t[6], t[7])}, rO, xo, 32 * 896, OUTAUX);
  }
}

extern "C" void kernel_launch(void* const* d_in, const int* in_sizes, int n_in, void* d_out, int out_size, void* d_ws, size_t ws_size,
                              hipStream_t stream) {
  (void)in_sizes; (void)n_in; (void)out_size; (void)ws_size;
  (void)hipFuncSetAttribute((const void*)fused_h3_mlp, hipFuncAttributeMaxDynamicSharedMemorySize, LDS_BYTES);
  fused_h3_mlp<<<dim3(8, 8, 4), 512, LDS_BYTES, stream>>>((const u16*)d_in[0], (const u16*)d_in[1], (const u16*)d_in[2], (const u16*)d_in[3],
                                                         (const u16*)d_in[4], (const u16*)d_in[5], (const u16*)d_in[7], (const u16*)d_in[8],
                                                         (const u16*)d_in[9], (const u16*)d_in[10], (u16*)d_out, (char*)d_ws);
}
```

```cpp
#include <hip/hip_runtime.h>
#include <hip/hip_bf16.h>
#include <stdint.h>
#include <type_traits>

typedef unsigned short u16;
typedef unsigned long long u64;
typedef __bf16 bf16x8 __attribute__((ext_vector_type(8)));
typedef __bf16 bf16x2 __attribute__((ext_vector_type(2)));
typedef short s16x2 __attribute__((ext_vector_type(2)));
typedef short s16x4 __attribute__((ext_vector_type(4)));
typedef float f32x2 __attribute__((ext_vector_type(2)));
typedef float f32x4 __attribute__((ext_vector_type(4)));
typedef float f32x16 __attribute__((ext_vector_type(16)));
typedef unsigned u32x2 __attribute__((ext_vector_type(2)));
typedef unsigned u32x4 __attribute__((ext_vector_type(4)));
typedef int i32x4 __attribute__((ext_vector_type(4)));
#define GAS __attribute__((address_space(1)))
#define LAS __attribute__((address_space(3)))
#define DI __device__ __forceinline__
#pragma clang diagnostic ignored "-Wint-to-pointer-cast"

constexpr int S_ = 2048, D_ = 448, F_ = 1792;
constexpr int NWG = 224;
constexpr unsigned FSTR = 256;
constexpr unsigned WS_EPOCH = 0;
constexpr unsigned WS_KVF = 4096;
constexpr unsigned WS_HF = WS_KVF + 1792 * FSTR;
constexpr unsigned WS_YF = WS_HF + 1792 * FSTR;
constexpr unsigned WS_W2F = WS_YF + 1792 * FSTR;
constexpr unsigned WS_STG = WS_W2F + 1792 * FSTR;
constexpr unsigned WS_CSG = WS_STG + 2048 * 8;
constexpr unsigned WS_KVT = 2u << 20;
constexpr unsigned WS_YT = 4u << 20;
constexpr unsigned WS_HT = 6u << 20;
constexpr unsigned WS_W2T = WS_HT + 64u * 112u * 1024u;
constexpr unsigned WS_END = WS_W2T + 7u * 112u * 2048u;
static_assert(WS_CSG + 1792 * 8 <= WS_KVT, "ws");
constexpr unsigned WS_DBG = 16u << 20;
constexpr unsigned L_X = 49152;
constexpr unsigned L_W = 0;
constexpr unsigned L_E = 98304;
constexpr unsigned E_SZ = 16640;
constexpr unsigned U_PL = 1568, U_SZ = 4 * U_PL;
constexpr unsigned L_SH = L_E + 2 * E_SZ;
constexpr unsigned L_SS = L_SH + 8192 + 64;
constexpr unsigned L_KV = L_SS + 1024;
constexpr unsigned L_Y = L_KV;
constexpr unsigned L_ST = L_KV + 9216;
constexpr unsigned L_U = L_ST + 1024;
constexpr unsigned P_SZ = 8704;
constexpr unsigned L_CP = 0;
constexpr unsigned L_G0 = 69632;
constexpr unsigned G_SZ = 8448;
constexpr unsigned L_G1 = 0;
constexpr unsigned L_SK = L_U;
constexpr unsigned L_TP = L_U + 9216;
constexpr unsigned L_SV = L_KV;
constexpr unsigned L_KO = L_KV;
constexpr unsigned L_SW = L_SH;
constexpr unsigned G_H = 528, G_G = 1056, G_T = 4224;
constexpr unsigned LDS_BYTES = L_U + 2 * U_SZ;
static_assert(L_G0 + 8 * G_SZ <= L_KV && L_U % 16 == 0, "lds");
static_assert(LDS_BYTES <= 163840, "lds");

constexpr unsigned SPIN_CAP = 1u << 18;
constexpr unsigned ONES = 0x3f803f80u, ONE_HI = 0x3f800000u;

DI float bflo(unsigned v) { return __uint_as_float(v << 16); }
DI float bfhi(unsigned v) { return __uint_as_float(v & 0xffff0000u); }
DI unsigned pk(float lo, float hi) { f32x2 f = {lo, hi}; return __builtin_bit_cast(unsigned, __builtin_convertvector(f, bf16x2)); }
DI float fmul1(float a, float b) { float d; asm("v_mul_f32 %0, %1, %2" : "=v"(d) : "v"(a), "v"(b)); return d; }
DI float rbf(float a) { return bfhi(pk(a, a)); }
DI float dot2(unsigned a, unsigned b, float c) { return __builtin_amdgcn_fdot2_f32_bf16(__builtin_bit_cast(bf16x2, a), __builtin_bit_cast(bf16x2, b), c, false); }
DI void dot2x8(float (&d)[8], const unsigned (&a)[8], unsigned b0, unsigned b1, unsigned b2, unsigned b3) {
  asm volatile("v_dot2_f32_bf16 %0, %8, %16, 0\n\tv_dot2_f32_bf16 %1, %9, %16, 0\n\tv_dot2_f32_bf16 %2, %10, %17, 0\n\tv_dot2_f32_bf16 %3, %11, %17, 0\n\t"
               "v_dot2_f32_bf16 %4, %12, %18, 0\n\tv_dot2_f32_bf16 %5, %13, %18, 0\n\tv_dot2_f32_bf16 %6, %14, %19, 0\n\tv_dot2_f32_bf16 %7, %15, %19, 0\n\ts_nop 2"
               : "=&v"(d[0]), "=&v"(d[1]), "=&v"(d[2]), "=&v"(d[3]), "=&v"(d[4]), "=&v"(d[5]), "=&v"(d[6]), "=&v"(d[7])
               : "v"(a[0]), "v"(a[1]), "v"(a[2]), "v"(a[3]), "v"(a[4]), "v"(a[5]), "v"(a[6]), "v"(a[7]), "v"(b0), "v"(b1), "v"(b2), "v"(b3));
}
DI f32x16 mfma(u32x4 a, u32x4 b, f32x16 c) { return __builtin_amdgcn_mfma_f32_32x32x16_bf16(__builtin_bit_cast(bf16x8, a), __builtin_bit_cast(bf16x8, b), c, 0, 0, 0); }
DI u32x4 lds128(unsigned a) { return *(const LAS u32x4*)a; }
DI u32x2 lds64(unsigned a) { return *(const LAS u32x2*)a; }
DI u32x2 ldstr(unsigned a) { return __builtin_bit_cast(u32x2, __builtin_amdgcn_ds_read_tr16_b64_v4i16((LAS s16x4*)a)); }
DI void sts128(unsigned a, u32x4 v) { *(LAS u32x4*)a = v; }
DI i32x4 mksrd(const void* p, unsigned bytes) {
  u64 a = (u64)p;
  i32x4 r = {(int)(unsigned)a, (int)((unsigned)(a >> 32) & 0xffffu), (int)bytes, 0x00020000};
  return r;
}
#define VMWAIT(n) asm volatile("s_waitcnt vmcnt(%0)" ::"i"(n) : "memory")
#define BAR() asm volatile("s_waitcnt lgkmcnt(0)\n\ts_barrier" ::: "memory")
#define OPAQUE(x) asm volatile("" : "+v"(x))
#define SB() __builtin_amdgcn_sched_barrier(0)
#define ROR(x, n) __uint_as_float(__builtin_amdgcn_update_dpp(0u, __float_as_uint(x), 0x120 + (n), 0xf, 0xf, false))
#define PIN8(a) asm volatile("" : "+v"(a[0]), "+v"(a[1]), "+v"(a[2]), "+v"(a[3]), "+v"(a[4]), "+v"(a[5]), "+v"(a[6]), "+v"(a[7]))
template <int LOFF, int GOFF> DI void dma(unsigned wb, unsigned voff, i32x4 srd, unsigned soff) {
  asm volatile("s_add_i32 m0, %0, %4\n\ts_nop 0\n\tbuffer_load_dwordx4 %1, %2, %3 offen offset:%5 lds"
               ::"s"(wb), "v"(voff), "s"(srd), "s"(soff), "i"(LOFF - GOFF), "i"(GOFF) : "scc");
}
template <typename R> DI void st128_sc1(u32x4 v, R rsrc, unsigned voff, unsigned soff) {
  __builtin_amdgcn_raw_buffer_store_b128(v, rsrc, voff, soff, 16);
  asm volatile("s_nop 1" ::"v"(v) : "memory");
}
DI void flag_store(char* ws, unsigned off, u64 tag, int lane) {
  if (lane == 0) __hip_atomic_store((GAS u64*)(ws + off), tag, __ATOMIC_RELAXED, __HIP_MEMORY_SCOPE_AGENT);
}
DI void poll_flags(char* ws, unsigned off, u64 tag) {
#pragma unroll 1
  for (unsigned spins = 0; spins < SPIN_CAP; ++spins) {
    u64 f = __hip_atomic_load((GAS u64*)(ws + off), __ATOMIC_RELAXED, __HIP_MEMORY_SCOPE_AGENT);
    if (__all(f == tag)) break;
    __builtin_amdgcn_s_sleep(1);
  }
  asm volatile("" ::: "memory");
}

DI unsigned poll_gran(char* ws, unsigned off, unsigned tag) {
  u64 f = 0;
#pragma unroll 1
  for (unsigned spins = 0; spins < SPIN_CAP; ++spins) {
    f = __hip_atomic_load((GAS u64*)(ws + off), __ATOMIC_RELAXED, __HIP_MEMORY_SCOPE_AGENT);
    if (__all((unsigned)(f >> 32) == tag)) break;
    __builtin_amdgcn_s_sleep(1);
  }
  asm volatile("" ::: "memory");
  return (unsigned)f;
}

#define STAMP(i) do {} while (0)
#define PART42 1
#define SIGF 9
#define HF0 0
#define NOW2 0
#define KSW 5
#define HPOLL 12
#define HCHK 14
#define HPS 4
#define TAUX 16
#define HAUX 16
#define ABL 0
#define YPOLL 17
#define YLAG 6
#define OUTAUX 16
#define HFIRST 6
#define HWIN 12

__global__ __launch_bounds__(512, 2) void fused_h3_mlp(const u16* __restrict__ gx, const u16* __restrict__ gk, const u16* __restrict__ gv,
                                                      const u16* __restrict__ gshk, const u16* __restrict__ gssk, const u16* __restrict__ gsD,
                                                      const u16* __restrict__ gw1, const u16* __restrict__ gb1, const u16* __restrict__ gw2,
                                                      const u16* __restrict__ gb2, u16* __restrict__ gout, char* __restrict__ ws) {
  STAMP(0);
  const int g = blockIdx.x, yy = blockIdx.y, zz = blockIdx.z;
  const int w = __builtin_amdgcn_readfirstlane(threadIdx.x >> 6);
  const int lane = threadIdx.x & 63, r = lane & 31, hh = lane >> 5;
  const i32x4 sWS = mksrd(ws, WS_END);
  asm volatile("" ::"s"(gx), "s"(gk), "s"(gv), "s"(gshk), "s"(gssk), "s"(gsD), "s"(gw1), "s"(gb1), "s"(gw2), "s"(gb2), "s"(gout), "s"(ws));
  u64 tag;
  if (yy == 7) {
    const int a = g + 8 * zz;
    const i32x4 sX = mksrd(gx, S_ * D_ * 2), sW1 = mksrd(gw1, D_ * F_ * 2);
    u32x4 xr[8], wv[7];
    const int row0 = 256 * g + 64 * zz + 8 * w;
    const unsigned xo = lane < 56 && !(ABL & 4) ? lane * 16 : 0x7fffffffu;
#pragma unroll
    for (int j = 0; j < 8; ++j)
      asm volatile("buffer_load_dwordx4 %0, %1, %2, %3 offen" : "=v"(xr[j]) : "v"(xo), "s"(sX), "s"((row0 + j) * 896) : "memory");
    const int ci = (g >> 1) + 4 * zz, cb = 14 * (g & 1) + ci;
    const unsigned wo = ci < 14 && !(ABL & 4) ? (lane >> 3) * 3584 + (lane & 7) * 16 + cb * 128 : 0x7fffffffu;
#pragma unroll
    for (int j = 0; j < 7; ++j)
      asm volatile("buffer_load_dwordx4 %0, %1, %2, %3 offen" : "=v"(wv[j]) : "v"(wo), "s"(sW1), "s"((56 * w + 8 * j) * 3584) : "memory");
    asm volatile("s_load_dwordx2 %0, %1, 0x0\n\ts_waitcnt lgkmcnt(0)" : "=s"(tag) : "s"(ws) : "memory");
    tag += 1ull;
    {
      float v[16];
#pragma unroll
      for (int j = 0; j < 8; ++j) {
        asm volatile("s_waitcnt vmcnt(%1)" : "+v"(xr[j]) : "i"(14 - j) : "memory");
        float s = 0.f, q = 0.f;
#pragma unroll
        for (int e = 0; e < 4; ++e) { s = dot2(xr[j][e], ONES, s); q = dot2(xr[j][e], xr[j][e], q); }
        v[j] = s; v[8 + j] = q;
        asm volatile("" : "+v"(v[j]), "+v"(v[8 + j]));
      }
#pragma unroll
      for (int j = 0; j < 8; ++j) { auto t = __builtin_amdgcn_permlane32_swap(__float_as_uint(v[j]), __float_as_uint(v[8 + j]), false, false); v[j] = __uint_as_float(t[0]) + __uint_as_float(t[1]); }
#pragma unroll
      for (int j = 0; j < 4; ++j) { auto t = __builtin_amdgcn_permlane16_swap(__float_as_uint(v[j]), __float_as_uint(v[4 + j]), false, false); v[j] = __uint_as_float(t[0]) + __uint_as_float(t[1]); }
#pragma unroll
      for (int j = 0; j < 4; ++j) { v[j] += ROR(v[j], 8); v[j] += ROR(v[j], 4); v[j] += ROR(v[j], 2); v[j] += ROR(v[j], 1); }
      const float mine = (lane & 2) ? ((lane & 1) ? v[3] : v[2]) : ((lane & 1) ? v[1] : v[0]);
      const auto t = __builtin_amdgcn_permlane32_swap(__float_as_uint(mine), __float_as_uint(mine), false, false);
      const float sq = __uint_as_float(t[1]);
      const float mean = mine * (1.f / D_);
      const float var = fmaxf(sq * (1.f / D_) - mean * mean, 0.f);
      const float den = __builtin_amdgcn_sqrtf(rbf(rbf(var) + 1e-5f));
      if ((lane & 44) == 0)
        __hip_atomic_store((GAS u64*)(ws + WS_STG + (row0 + ((lane >> 4) & 1) * 4 + (lane & 3)) * 8), ((u64)(unsigned)tag << 32) | pk(mean, den), __ATOMIC_RELAXED, __HIP_MEMORY_SCOPE_AGENT);
    }
    {
      unsigned onelo = 0x3f80u; OPAQUE(onelo);
      float c[8];
#pragma unroll
      for (int e = 0; e < 8; ++e) c[e] = 0.f;
#pragma unroll
      for (int j = 0; j < 7; ++j) {
        asm volatile("s_waitcnt vmcnt(%1)" : "+v"(wv[j]) : "i"(7 - j) : "memory");
#pragma unroll
        for (int e = 0; e < 4; ++e) { c[2 * e] = dot2(wv[j][e], onelo, c[2 * e]); c[2 * e + 1] = dot2(wv[j][e], ONE_HI, c[2 * e + 1]); }
        PIN8(c);
      }
#pragma unroll
      for (int e = 0; e < 4; ++e) { auto t = __builtin_amdgcn_permlane32_swap(__float_as_uint(c[e]), __float_as_uint(c[4 + e]), false, false); c[e] = __uint_as_float(t[0]) + __uint_as_float(t[1]); }
#pragma unroll
      for (int e = 0; e < 2; ++e) { auto t = __builtin_amdgcn_permlane16_swap(__float_as_uint(c[e]), __float_as_uint(c[2 + e]), false, false); c[e] = __uint_as_float(t[0]) + __uint_as_float(t[1]); }
      c[0] += ROR(c[0], 8); c[1] += ROR(c[1], 8);
      *(LAS f32x2*)(w * 256 + (lane & 7) * 32 + (lane >> 4) * 8) = f32x2{c[0], c[1]};
      BAR();
      if (w == 0 && ci < 14) {
        float t = 0.f;
#pragma unroll
        for (int j = 0; j < 8; ++j) t += *(const LAS float*)(j * 256 + lane * 4);
        __hip_atomic_store((GAS u64*)(ws + WS_CSG + (64 * cb + lane) * 8), ((u64)(unsigned)tag << 32) | __float_as_uint(t), __ATOMIC_RELAXED, __HIP_MEMORY_SCOPE_AGENT);
      }
    }
    if (a == 31) {
#pragma unroll 1
      for (int j = 0; j < 4; ++j) poll_gran(ws, WS_STG + ((w * 4 + j) * 64 + lane) * 8, (unsigned)tag);
#pragma unroll 1
      for (unsigned spins = 0; spins < SPIN_CAP; ++spins) {
        bool ok = true;
#pragma unroll
        for (int j = 0; j < 4; ++j) {
          const unsigned i = (w * 4 + j) * 64 + lane;
          ok &= __hip_atomic_load((GAS u64*)(ws + WS_HF + (i < 1792 ? i : 0) * FSTR), __ATOMIC_RELAXED, __HIP_MEMORY_SCOPE_AGENT) == tag;
        }
        if (__all(ok)) break;
        __builtin_amdgcn_s_sleep(8);
      }
      BAR();
      if (w == 0) flag_store(ws, WS_EPOCH, tag, lane);
    }
    return;
  }
  const int mt128 = 4 * (g >> 1) + zz, ft = 7 * (g & 1) + yy, hsel = g & 1;
  const int mt64 = 2 * mt128 + hsel, nt = yy;
  const int fg = w & 3, tg = w >> 2;
  const int cg = yy, tt = g + 8 * zz;
  const int ch0 = 64 * cg + 2 * tt;

  const i32x4 sX = mksrd(gx, S_ * D_ * 2), sW1 = mksrd(gw1, D_ * F_ * 2);
  unsigned vx0, vx1, vw0, vw1;
  {
    const int row = 16 * w + (lane >> 3);
    vx0 = (128 * mt128 + row) * 896 + (((lane ^ (lane >> 4)) & 7) << 4);
    vx1 = (vx0 + 8 * 896) ^ 64;
    const int q = lane >> 4;
    vw0 = (8 * w + q) * 3584 + ft * 256 + (((lane & 15) ^ ((q << 2) | (2 * (w & 1)))) << 4);
    vw1 = (vw0 + 4 * 3584) ^ 16;
  }
  const unsigned wb = w * 2048, wb1 = w * 1024;
#define SLX(k) ((k) == 3 ? L_E + 16384 : L_X + ((k) & 3) * 16384)
#define SLW(k) ((k) == 3 ? L_E : L_W + ((k) & 3) * 16384)
#define DMA1(k, j) do { \
    if ((j) == 0) dma<SLX(k), (k) * 128>(wb, vx0, sX, 0); \
    if ((j) == 1) dma<SLW(k), 0>(wb, vw0, sW1, (k) * 64 * 3584); \
    if ((j) == 2) dma<SLX(k) + 1024, (k) * 128>(wb, vx1, sX, 0); \
    if ((j) == 3) dma<SLW(k) + 1024, 0>(wb, vw1, sW1, (k) * 64 * 3584); } while (0)
  DMA1(0, 0); DMA1(0, 1); DMA1(0, 2); DMA1(0, 3);
  __builtin_amdgcn_s_barrier();
  const unsigned vr8 = (lane >> 3) * 896 + (lane & 7) * 16;
  {
    const unsigned kvso = 64 * tt * 896 + cg * 128;
    const i32x4 sK = mksrd(gk, S_ * D_ * 2);
    if (w == 4) dma<L_SK, 0>(0, tt ? vr8 : 0x7fffffffu, sK, tt ? kvso - 8 * 896 : 0);
    if (w == 5) dma<L_TP, 0>(0, lane * 16, mksrd(gshk, D_ * 8), cg * 512);
    dma<L_SK + 1024, 0>(wb1, vr8, sK, kvso + 8 * w * 896);
    dma<L_SV, 0>(wb1, vr8, mksrd(gv, S_ * D_ * 2), kvso + 8 * w * 896);
  }
  DMA1(1, 0); DMA1(1, 1); DMA1(1, 2); DMA1(1, 3);
  const auto rWS = __builtin_amdgcn_make_buffer_rsrc(ws, 0, WS_END, 0x00020000);
  unsigned dpair;
  asm volatile("s_load_dwordx2 %0, %2, 0x0\n\ts_load_dword %1, %3, 0x0" : "=&s"(tag), "=&s"(dpair) : "s"(ws), "s"(gsD + ch0) : "memory");

  unsigned aA0, aA1, aB[4];
  const int blk = (lane >> 4) & 1, q4 = (lane & 15) >> 2, p4 = lane & 3;
  {
    const int ch = 4 * fg + 2 * blk + (p4 >> 1);
    aA0 = L_W + 256 * (8 * hh + q4) + 16 * (ch ^ ((q4 << 2) | (2 * hh))) + 8 * (p4 & 1);
    aA1 = (aA0 + 1024) ^ 16;
    const unsigned b0 = L_X + (64 * tg + r) * 128 + ((hh ^ ((r >> 1) & 7)) << 4);
#pragma unroll
    for (int j = 0; j < 4; ++j) { aB[j] = b0 ^ (j << 5); OPAQUE(aB[j]); }
    OPAQUE(aA0); OPAQUE(aA1);
  }
  f32x16 acc0, acc1;
#pragma unroll
  for (int i = 0; i < 16; ++i) { acc0[i] = 0.f; acc1[i] = 0.f; }
  u32x4 fA[2], fB0[2], fB1[2];
  auto ldf = [&](int n) {
    const int j = n & 3, k = n >> 2;
    unsigned pa0 = aA0, pa1 = aA1, pb = aB[j];
    if (k == 3) { pa0 += L_E - L_W; pa1 += L_E - L_W; pb += L_E + 16384 - L_X; OPAQUE(pa0); OPAQUE(pa1); OPAQUE(pb); }
    const unsigned so = k == 3 ? 0 : (k & 3) * 16384;
    u32x2 a0 = ldstr(pa0 + so + j * 4096), a1 = ldstr(pa1 + so + j * 4096);
    fA[n & 1] = u32x4{a0[0], a0[1], a1[0], a1[1]};
    fB0[n & 1] = lds128(pb + so); fB1[n & 1] = lds128(pb + so + 4096);
  };
#define STEP(n) do { \
    if ((n) + 1 < 28) ldf((n) + 1); \
    SB(); \
    acc0 = mfma(fA[(n) & 1], fB0[(n) & 1], acc0); \
    acc1 = mfma(fA[(n) & 1], fB1[(n) & 1], acc1); \
    asm volatile("" : "+v"(acc0), "+v"(acc1)); \
    SB(); \
  } while (0)
#define PIECE(m) DMA1(((m) >> 2) + 2, (m) & 3)
  STAMP(1);
  asm volatile("s_waitcnt vmcnt(6) lgkmcnt(0)\n\ts_barrier" : "+s"(tag), "+s"(dpair) :: "memory");
  tag += 1ull;
  STAMP(2);
  ldf(0);
  dma<L_SW, 0>(wb1, vr8, mksrd(gw2, tt < 28 ? D_ * F_ * 2 : 0), (64 * tt + 8 * w) * 896 + cg * 128);
  STEP(0); PIECE(0); STEP(1); PIECE(1); STEP(2); PIECE(2);
  VMWAIT(3); BAR();
  STAMP(3);
  u32x4 w2u;
  {
    const unsigned ta = (8 * w + q4) * 128 + (lane >> 4) * 32 + p4 * 8;
    u32x2 k0 = ldstr(L_SK + 512 + ta), k1 = ldstr(L_SK + 1024 + ta), k2 = ldstr(L_SK + 1536 + ta);
    u32x2 v0 = ldstr(L_SV + ta), v1 = ldstr(L_SV + 512 + ta);
    u32x2 shk = lds64(L_TP + lane * 8);
    const unsigned wa = L_SW + (16 * (w >> 1) + 4 * hh + q4) * 128 + (w & 1) * 64 + blk * 32 + p4 * 8;
    const u32x2 wa0 = ldstr(wa), wa1 = ldstr(wa + 1024);
    w2u = u32x4{wa0[0], wa0[1], wa1[0], wa1[1]};
    STEP(3);
    PIECE(3);
    BAR();
    PIECE(4);
    const float kr[11] = {bfhi(k0[0]), bflo(k0[1]), bfhi(k0[1]), bflo(k1[0]), bfhi(k1[0]), bflo(k1[1]), bfhi(k1[1]), bflo(k2[0]), bfhi(k2[0]), bflo(k2[1]), bfhi(k2[1])};
    const float tw0 = bflo(shk[0]), tw1 = bfhi(shk[0]), tw2 = bflo(shk[1]), tw3 = bfhi(shk[1]);
    float a[8]; unsigned pp[8];
#pragma unroll
    for (int j = 0; j < 8; ++j) pp[j] = pk(fmul1(kr[j], tw3), fmul1(kr[j + 1], tw2));
    PIECE(5);
    dot2x8(a, pp, ONES, ONES, ONES, ONES);
#pragma unroll
    for (int j = 0; j < 8; ++j) pp[j] = pk(a[j], fmul1(kr[j + 2], tw1));
    PIECE(6);
    dot2x8(a, pp, ONES, ONES, ONES, ONES);
#pragma unroll
    for (int j = 0; j < 8; ++j) pp[j] = pk(a[j], fmul1(kr[j + 3], tw0));
    PIECE(7);
    dot2x8(a, pp, ONES, ONES, ONES, ONES);
#pragma unroll
    for (int j = 0; j < 8; j += 2) { pp[j] = pk(a[j], 0.f); pp[j + 1] = pk(0.f, a[j + 1]); }
    dot2x8(a, pp, v0[0], v0[1], v1[0], v1[1]);
    sts128(L_KO + lane * 128 + ((w ^ (lane & 7)) << 4), u32x4{pk(a[7], a[6]), pk(a[5], a[4]), pk(a[3], a[2]), pk(a[1], a[0])});
    SB();
  }
  STAMP(20);
  STEP(4); PIECE(8); STEP(5); PIECE(9); STEP(6); PIECE(10);
  VMWAIT(7); BAR();
  STAMP(4);
  {
    const u32x4 o = lds128(L_KO + (8 * w + (lane >> 3)) * 128 + (((lane ^ (lane >> 3)) & 7) << 4));
    st128_sc1(w2u, rWS, lane * 16, tt < 28 ? WS_W2T + (cg * 112 + 4 * tt) * 2048 + w * 1024 : WS_END);
    STEP(7);
    st128_sc1(o, rWS, (64 * cg + 8 * w + (lane >> 3)) * 4096 + (lane & 7) * 16, WS_KVT + tt * 128);
  }
  bool ksent = false;
#define TRYK(Y, FORCE) do { if (!ksent) { const unsigned ib = __builtin_amdgcn_s_getreg(7 | (31 << 11)); \
      if ((FORCE) || ((ib & 15u) | ((ib >> 18) & 48u)) <= (Y)) { VMWAIT(Y); flag_store(ws, WS_KVF + ((cg * 32 + tt) * 8 + w) * FSTR, tag, lane); ksent = true; } } } while (0)
  PIECE(11); STEP(8); PIECE(12); STEP(9); PIECE(13); STEP(10); PIECE(14);
  VMWAIT(9); TRYK(4, false); BAR();
  STAMP(5);
  STEP(11); PIECE(15); TRYK(5, false); STEP(12); PIECE(16); TRYK(6, false); STEP(13); PIECE(17); TRYK(7, false); STEP(14); PIECE(18);
  VMWAIT(7); TRYK(8, true); BAR();
  STAMP(6);
  STEP(15); PIECE(19);
  STEP(16); dma<L_SH, 0>(wb1, lane * 16, mksrd(gssk, S_ * D_ * 2), ch0 * 4096 + wb1);
  STEP(17);
  if (w < 4) sts128(L_U + (w >> 1) * U_SZ + ((w & 1) * 2 + hh) * U_PL + r * 16, u32x4{0u, 0u, 0u, 0u});
  const unsigned kvf_off = lane < 8 ? WS_KVF + (cg * 256 + (8 * fg + lane) * 8 + (tt >> 2)) * FSTR : WS_KVF + (cg * 256 + 28 * w + (lane < 36 ? lane - 8 : 0)) * FSTR;
  const unsigned gs_off = WS_STG + (128 * mt128 + 64 * tg + lane) * 8, gc_off = WS_CSG + (128 * ft + 32 * fg + r) * 8;
  u32x2 pf, gs, gc; unsigned b1v = 0; u32x4 uld;
  STEP(18);
  VMWAIT(5); BAR();
  STAMP(7);
  STEP(19);
  asm volatile("buffer_load_dwordx2 %0, %1, %2, 0 offen sc1" : "=v"(pf) : "v"(kvf_off), "s"(sWS) : "memory");
  asm volatile("global_load_short_d16_hi %0, %1, %2" : "+v"(b1v) : "v"((128 * ft + 32 * fg + r) * 2), "s"(gb1) : "memory");
  STEP(20); STEP(21); STEP(22);
  VMWAIT(2); BAR();
  STAMP(8);
  STEP(23);
  asm volatile("buffer_load_dwordx2 %0, %1, %2, 0 offen sc1" : "=v"(gs) : "v"(gs_off), "s"(sWS) : "memory");
  asm volatile("buffer_load_dwordx2 %0, %1, %2, 0 offen sc1" : "=v"(gc) : "v"(gc_off), "s"(sWS) : "memory");
  unsigned ed[4][3];
  {
    unsigned ra = L_SH + w * 1024 + lane * 4;
    OPAQUE(ra);
#pragma unroll
    for (int j = 0; j < 4; ++j)
#pragma unroll
      for (int e = 0; e < 3; ++e) ed[j][e] = *(const LAS unsigned*)(ra + j * 256 + e * 4);
  }
  STEP(24);
  asm volatile("s_waitcnt vmcnt(2)" : "+v"(pf), "+v"(b1v) :: "memory");
  if (!__all((((u64)pf[1] << 32) | pf[0]) == tag)) poll_flags(ws, kvf_off, tag);
  asm volatile("buffer_load_dwordx4 %0, %1, %2, %3 offen sc1" : "=v"(uld) : "v"(lane * 16), "s"(sWS), "s"(WS_KVT + (ch0 + tg) * 4096 + fg * 1024) : "memory");
  u32x4 wf[28];
  unsigned l16 = lane * 16; OPAQUE(l16);
  const unsigned w2so = WS_W2T + (nt * 112 + 14 * w) * 2048;
  auto ldw2 = [&](int n0, int n1) {
#pragma unroll
    for (int n = n0; n < n1; ++n) { wf[n] = __builtin_amdgcn_raw_buffer_load_b128(rWS, (ABL & 16) ? 0x7fffffffu : l16 + (n & 3) * 1024, w2so + (n >> 2) * 4096, TAUX); SB(); }
  };
  STEP(25); ldw2(0, 2);
  STEP(26); ldw2(2, 4);
  STEP(27); ldw2(4, 5);
  asm volatile("s_waitcnt vmcnt(6)" : "+v"(gs), "+v"(gc) :: "memory");
  if (!__all(gs[1] == (unsigned)tag && gc[1] == (unsigned)tag)) { gs[0] = poll_gran(ws, gs_off, (unsigned)tag); gc[0] = poll_gran(ws, gc_off, (unsigned)tag); }
  STAMP(10);
  {
    if (fg == 0) *(LAS unsigned*)(L_ST + (64 * tg + lane) * 4) = gs[0];
    const unsigned nm = gs[0] ^ 0x8000u;
    const unsigned bm = __builtin_amdgcn_perm(nm, nm, 0x01000100u);
    const float c = __uint_as_float(gc[0]), chi = rbf(c), c1 = c - chi, cmid = rbf(c1);
    const u32x4 A = {pk(chi, cmid), pk(c1 - cmid, __uint_as_float(b1v)), 0u, 0u};
    acc0 = mfma(A, u32x4{hh ? 0u : bm, hh ? 0u : nm, 0u, 0u}, acc0);
    acc1 = mfma(A, u32x4{hh ? bm : 0u, hh ? nm : 0u, 0u, 0u}, acc1);
    const unsigned so = WS_HT + ((4 * mt128 + 2 * tg) * 112 + 8 * ft + 2 * fg) * 1024;
    unsigned wa = L_E + 256 + tg * E_SZ + fg * 4096 + lane * 16;
    OPAQUE(wa);
#pragma unroll
    for (int q = 0; q < 4; ++q) {
      sts128(wa + q * 1024, u32x4{ed[q][0], ed[q][1], __builtin_amdgcn_alignbit(ed[q][1], ed[q][0], 16), __builtin_amdgcn_alignbit(ed[q][2], ed[q][1], 16)});
      u32x4 hv;
#pragma unroll
      for (int e = 0; e < 4; ++e) {
        float a0 = (q < 2 ? acc0 : acc1)[8 * (q & 1) + 2 * e], a1 = (q < 2 ? acc0 : acc1)[8 * (q & 1) + 2 * e + 1];
        a0 *= __builtin_fabsf(a0); OPAQUE(a0);
        a1 *= __builtin_fabsf(a1);
        const s16x2 m = __builtin_elementwise_max(__builtin_bit_cast(s16x2, pk(a0, a1)), s16x2{0, 0});
        hv[e] = __builtin_bit_cast(unsigned, m);
      }
      st128_sc1(hv, rWS, (ABL & 8) ? 0x7fffffffu : l16 + (q & 1) * 1024, so + (q >> 1) * 112 * 1024);
    }
  }
  STAMP(11);
  asm volatile("s_waitcnt vmcnt(9)" : "+v"(uld) :: "memory");
  {
    const int n = 64 * fg + lane;
    sts128(L_U + tg * U_SZ + (n & 3) * U_PL + ((n >> 2) + 32) * 16, uld);
    if (fg == 0) {
      const unsigned h0 = __builtin_amdgcn_readfirstlane(ed[0][0]), h1 = __builtin_amdgcn_readfirstlane(__builtin_amdgcn_alignbit(ed[0][1], ed[0][0], 16));
      const int l = lane & 15;
      sts128(L_E + tg * E_SZ + l * 16, u32x4{0u, l == 15 ? h0 : 0u, l == 15 ? h0 << 16 : 0u, l == 15 ? h1 : l == 14 ? h0 << 16 : 0u});
    }
  }
  ldw2(5, 7);
  BAR();
  STAMP(21);
  const int tok = threadIdx.x >> 4, quad = threadIdx.x & 15;
  const unsigned hf_off = WS_HF + (mt64 * 56 + 7 * w + (lane < 7 ? lane : 6)) * FSTR;
  const unsigned xo = ((64 * mt64 + tok) * D_ + 64 * nt + 4 * quad) * 2;
  u32x2 hf, xr0, xr1, b2r;
  constexpr int WIN = HWIN, NH0 = (16 - HCHK) * HPS;
  u32x4 hb[WIN];
  const unsigned hso = WS_HT + (2 * mt64 * 112 + 14 * w) * 1024;
  auto ldh = [&](int n) {
    const int mt = n / 14, ks = n % 14;
    hb[n % WIN] = __builtin_amdgcn_raw_buffer_load_b128(rWS, (ABL & 32) ? 0x7fffffffu : l16 + (ks & 3) * 1024, hso + mt * 112 * 1024 + (ks >> 2) * 4096, HAUX);
    SB();
  };
  {
    f32x16 c0, c1;
#pragma unroll
    for (int i = 0; i < 16; ++i) { c0[i] = 0.f; c1[i] = 0.f; }
    unsigned ea0 = L_E + tg * E_SZ + 8 * (32 + r - 8 * hh - 7 + 256 * fg - 16), ea1 = ea0 + 32, ea2 = ea0 + 8192, ea3 = ea0 + 8224;
    unsigned ua = L_U + tg * U_SZ + hh * U_PL + 16 * (r + 32 - 8 * fg - 7);
    OPAQUE(ea0); OPAQUE(ea1); OPAQUE(ea2); OPAQUE(ea3); OPAQUE(ua);
    u32x4 A0[2], A1[2], B0[2], B1[2];
    auto ldc = [&](int st) {
      const int dd = st >> 1, s = st & 1;
      const unsigned eo = 8 * (32 * dd + 16 * (1 - s)), uo = 2 * s * U_PL + 16 * (7 - dd);
      u32x2 x0 = lds64(ea0 + eo), x1 = lds64(ea1 + eo);
      B0[st & 1] = lds128(ua + uo); B1[st & 1] = lds128(ua + uo + 512);
      u32x2 y0 = lds64(ea2 + eo), y1 = lds64(ea3 + eo);
      A0[st & 1] = u32x4{x0[0], x0[1], x1[0], x1[1]}; A1[st & 1] = u32x4{y0[0], y0[1], y1[0], y1[1]};
    };
    ldc(0);
    SB();
    bool hsent = false;
#pragma unroll
    for (int st = 0; st < 16; ++st) {
      if (st + 1 < 16) ldc(st + 1);
      SB();
      c0 = mfma(A0[st & 1], B0[st & 1], c0);
      c1 = mfma(A0[st & 1], B1[st & 1], c1);
      c1 = mfma(A1[st & 1], B0[st & 1], c1);
      asm volatile("" : "+v"(c0), "+v"(c1));
      SB();
      if (st >= HF0 && st <= SIGF && !hsent) {
        const int younger = 2 + (st < 9 ? 2 * st : 18 + (st - 9));
        const unsigned ib = __builtin_amdgcn_s_getreg(7 | (31 << 11));
        if (st == SIGF || ((ib & 15u) | ((ib >> 18) & 48u)) <= (unsigned)younger) {
          VMWAIT(younger);
          flag_store(ws, WS_HF + ((2 * mt128 + tg) * 56 + 4 * ft + fg) * FSTR, tag, lane);
          hsent = true;
        }
      }
      if (st < 9) ldw2(7 + 2 * st, 9 + 2 * st); else if (st < 12) ldw2(16 + st, 17 + st);
      if (st == 12) {
        asm volatile("buffer_load_dwordx2 %0, %1, %2, 0 offen" : "=v"(xr0) : "v"(xo), "s"(sX) : "memory");
        asm volatile("buffer_load_dwordx2 %0, %1, %2, %3 offen" : "=v"(xr1) : "v"(xo), "s"(sX), "s"(32 * 896) : "memory");
        asm volatile("global_load_dwordx2 %0, %1, %2" : "=v"(b2r) : "v"((64 * nt + 4 * quad) * 2), "s"(gb2) : "memory");
      }
      if (st == HPOLL) asm volatile("buffer_load_dwordx2 %0, %1, %2, 0 offen sc1" : "=v"(hf) : "v"(hf_off), "s"(sWS) : "memory");
      if (st == HCHK) {
        STAMP(9);
        asm volatile("s_waitcnt vmcnt(0)" : "+v"(hf), "+v"(xr0), "+v"(xr1), "+v"(b2r) :: "memory");
        if (!__all((((u64)hf[1] << 32) | hf[0]) == tag)) poll_flags(ws, hf_off, tag);
        STAMP(22);
      }
      if (st >= HCHK) {
#pragma unroll
        for (int n = (st - HCHK) * HPS; n < (st - HCHK + 1) * HPS; ++n) ldh(n);
      }
    }
    STAMP(12);
    const unsigned pb = L_CP + w * P_SZ + lane * 16;
#pragma unroll
    for (int g4 = 0; g4 < 4; ++g4) {
      *(LAS f32x4*)(pb + g4 * 1088) = f32x4{c0[4 * g4], c0[4 * g4 + 1], c0[4 * g4 + 2], c0[4 * g4 + 3]};
      *(LAS f32x4*)(pb + 4352 + g4 * 1088) = f32x4{c1[4 * g4], c1[4 * g4 + 1], c1[4 * g4 + 2], c1[4 * g4 + 3]};
    }
  }
  BAR();
  STAMP(13);
  {
    const int n = 32 * w + r, a = n >> 2, g4 = n & 3;
    const unsigned po = L_CP + hh * 4 * P_SZ + (a >> 5) * 4352 + g4 * 1088 + (a & 31) * 16;
    f32x4 lo = *(const LAS f32x4*)po, hi = *(const LAS f32x4*)(po + 512);
#pragma unroll
    for (int q = 1; q < 4; ++q) {
      lo += *(const LAS f32x4*)(po + q * P_SZ);
      hi += *(const LAS f32x4*)(po + q * P_SZ + 512);
    }
    const u32x4 kvc = lds128(L_U + hh * U_SZ + g4 * U_PL + (a + 32) * 16);
    const unsigned dlo = hh ? dpair >> 16 : dpair & 0xffffu, dhi = dlo << 16;
    float pd[8], yv[8];
    { const unsigned kk[8] = {kvc[3], kvc[2], kvc[1], kvc[0], kvc[3], kvc[2], kvc[1], kvc[0]}; dot2x8(pd, kk, dhi, dhi, dlo, dlo); }
    { const unsigned pp[8] = {pk(lo[0], pd[0]), pk(lo[1], pd[4]), pk(lo[2], pd[1]), pk(lo[3], pd[5]), pk(hi[0], pd[2]), pk(hi[1], pd[6]), pk(hi[2], pd[3]), pk(hi[3], pd[7])};
      dot2x8(yv, pp, ONES, ONES, ONES, ONES); }
    const u32x4 yo = {pk(yv[0], yv[1]), pk(yv[2], yv[3]), pk(yv[4], yv[5]), pk(yv[6], yv[7])};
    st128_sc1(yo, rWS, (ch0 + hh) * 4096 + n * 16, WS_YT);
  }
  STAMP(14);
#pragma unroll
  for (int n = NH0; n < WIN; ++n) ldh(n);
  auto gate = [&](unsigned ra, float r2, unsigned (&gt)[2]) {
    f32x4 sm = *(const LAS f32x4*)ra;
#pragma unroll
    for (int q = 1; q < 8; ++q) sm += *(const LAS f32x4*)(ra + q * G_SZ);
    const unsigned oa = pk(fmaf(sm[0], r2, bflo(b2r[0])), fmaf(sm[1], r2, bfhi(b2r[0]))), ob = pk(fmaf(sm[2], r2, bflo(b2r[1])), fmaf(sm[3], r2, bfhi(b2r[1])));
    const float o[4] = {bflo(oa), bfhi(oa), bflo(ob), bfhi(ob)};
    float sv[4];
#pragma unroll
    for (int e = 0; e < 4; ++e) sv[e] = o[e] * __builtin_amdgcn_rcpf(1.f + __builtin_amdgcn_exp2f(o[e] * -1.4426950408889634f));
    gt[0] = pk(sv[0], sv[1]); gt[1] = pk(sv[2], sv[3]);
  };
  auto xnorm = [&](u32x2 xr, unsigned st, unsigned (&xn)[2], float& r2, float* xf) {
    const float m = bflo(st), rd = __builtin_amdgcn_rcpf(bfhi(st));
    xf[0] = bflo(xr[0]); xf[1] = bfhi(xr[0]); xf[2] = bflo(xr[1]); xf[3] = bfhi(xr[1]);
    const unsigned da = pk(xf[0] - m, xf[1] - m), db = pk(xf[2] - m, xf[3] - m);
    xn[0] = pk(bflo(da) * rd, bfhi(da) * rd); xn[1] = pk(bflo(db) * rd, bfhi(db) * rd);
    r2 = rd * rd;
  };
  unsigned g0[2], g1[2], xn0[2], xn1[2];
  float r20, r21, xf[8];
  unsigned rp0 = L_G0 + quad * G_H + tok * 16, rp1 = rp0 - (L_G0 - L_G1), yra = L_Y + 4 * quad * 132 + tok * 2;
  const unsigned yf_off = WS_YF + ((nt * 8 + (mt64 >> 2)) * 32 + 4 * w + (lane & 3)) * FSTR;
  u32x2 yf; u32x4 yb;
  const unsigned yvo = (lane >> 3) * 4096 + (lane & 7) * 16, yso = WS_YT + (64 * nt + 8 * w) * 4096 + mt64 * 128;
  STAMP(15);
  {
    f32x16 ca[2], cb[2];
#pragma unroll
    for (int i = 0; i < 16; ++i) { ca[0][i] = 0.f; cb[0][i] = 0.f; ca[1][i] = 0.f; cb[1][i] = 0.f; }
    unsigned pb0 = L_G0 + w * G_SZ + hh * G_H + r * 16, pb1 = pb0 - (L_G0 - L_G1);
    OPAQUE(pb0); OPAQUE(pb1);
    auto wrp = [&](unsigned pb, const f32x16& c, int g4, int t) { *(LAS f32x4*)(pb + t * G_T + g4 * G_G) = f32x4{c[4 * g4], c[4 * g4 + 1], c[4 * g4 + 2], c[4 * g4 + 3]}; };
#pragma unroll
    for (int n = 0; n < 28; ++n) {
      const int mt = n / 14, ks = n % 14;
      if (n == NH0) { VMWAIT(WIN); flag_store(ws, WS_YF + ((cg * 8 + w) * 32 + tt) * FSTR, tag, lane); }
      ca[mt] = mfma(wf[2 * ks], hb[n % WIN], ca[mt]);
      cb[mt] = mfma(wf[2 * ks + 1], hb[n % WIN], cb[mt]);
      asm volatile("" : "+v"(ca[mt]), "+v"(cb[mt]));
      SB();
      if (n + WIN < 28) ldh(n + WIN);
      if (n == 1) {
        const unsigned st0 = *(const LAS unsigned*)(L_ST + (64 * hsel + tok) * 4), st1 = *(const LAS unsigned*)(L_ST + (64 * hsel + 32 + tok) * 4);
        xnorm(xr0, st0, xn0, r20, xf); xnorm(xr1, st1, xn1, r21, xf + 4);
        asm volatile("" : "+v"(xn0[0]), "+v"(xn0[1]), "+v"(xn1[0]), "+v"(xn1[1]), "+v"(r20), "+v"(r21), "+v"(rp0), "+v"(rp1), "+v"(yra)); PIN8(xf); SB();
      }
      if (n >= 14 && n < 18) { wrp(pb0, ca[0], n - 14, 0); wrp(pb0, cb[0], n - 14, 1); SB(); }
      if (n == 20) { BAR(); STAMP(16); }
      if (n == YPOLL) asm volatile("buffer_load_dwordx2 %0, %1, %2, 0 offen sc1" : "=v"(yf) : "v"(yf_off), "s"(sWS) : "memory");
      if (n == YPOLL + YLAG) {
        asm volatile("s_waitcnt vmcnt(0)" : "+v"(yf) :: "memory");
        asm volatile("buffer_load_dwordx4 %0, %1, %2, %3 offen sc1" : "=v"(yb) : "v"(yvo), "s"(sWS), "s"(yso) : "memory");
      }
    }
#pragma unroll
    for (int g4 = 0; g4 < 4; ++g4) { wrp(pb1, ca[1], g4, 0); wrp(pb1, cb[1], g4, 1); }
    SB();
    gate(rp0, r20, g0);
  }
  STAMP(17);
  asm volatile("s_waitcnt vmcnt(0)" : "+v"(yb) :: "memory");
  if (!__all((((u64)yf[1] << 32) | yf[0]) == tag)) {
    poll_flags(ws, yf_off, tag);
    asm volatile("buffer_load_dwordx4 %0, %1, %2, %3 offen sc1\n\ts_waitcnt vmcnt(0)" : "=&v"(yb) : "v"(yvo), "s"(sWS), "s"(yso) : "memory");
  }
  {
    const unsigned ya = L_Y + (8 * w + (lane >> 3)) * 132 + (lane & 7) * 16;
#pragma unroll
    for (int e = 0; e < 4; ++e) *(LAS unsigned*)(ya + 4 * e) = yb[e];
  }
  BAR();
  STAMP(18);
  {
    unsigned yv[8];
#pragma unroll
    for (int e = 0; e < 8; ++e) yv[e] = *(const LAS u16*)(yra + (e & 3) * 132 + (e >> 2) * 64);
    gate(rp1, r21, g1);
    float t[8]; unsigned pp[8];
#pragma unroll
    for (int e = 1; e < 8; e += 2) yv[e] <<= 16;
    dot2x8(t, yv, xn0[0], xn0[1], xn1[0], xn1[1]);
#pragma unroll
    for (int e = 0; e < 8; e += 2) { pp[e] = pk(t[e], 0.f); pp[e + 1] = pk(0.f, t[e + 1]); }
    dot2x8(t, pp, g0[0], g0[1], g1[0], g1[1]);
#pragma unroll
    for (int e = 0; e < 8; ++e) pp[e] = pk(t[e], xf[e]);
    dot2x8(t, pp, ONES, ONES, ONES, ONES);
    const auto rO = __builtin_amdgcn_make_buffer_rsrc(gout, 0, S_ * D_ * 2, 0x00020000);
    __builtin_amdgcn_raw_buffer_store_b64(u32x2{pk(t[0], t[1]), pk(t[2], t[3])}, rO, xo, 0, OUTAUX);
    __builtin_amdgcn_raw_buffer_store_b64(u32x2{pk(t[4], t[5]), pk(t[6], t[7])}, rO, xo, 32 * 896, OUTAUX);
  }
}

extern "C" void kernel_launch(void* const* d_in, const int* in_sizes, int n_in, void* d_out, int out_size, void* d_ws, size_t ws_size,
                              hipStream_t stream) {
  (void)in_sizes; (void)n_in; (void)out_size; (void)ws_size;
  (void)hipFuncSetAttribute((const void*)fused_h3_mlp, hipFuncAttributeMaxDynamicSharedMemorySize, LDS_BYTES);
  fused_h3_mlp<<<dim3(8, 8, 4), 512, LDS_BYTES, stream>>>((const u16*)d_in[0], (const u16*)d_in[1], (const u16*)d_in[2], (const u16*)d_in[3],
                                                         (const u16*)d_in[4], (const u16*)d_in[5], (const u16*)d_in[7], (const u16*)d_in[8],
                                                         (const u16*)d_in[9], (const u16*)d_in[10], (u16*)d_out, (char*)d_ws);
}
```

```cpp
#include <hip/hip_runtime.h>
#include <hip/hip_bf16.h>
#include <stdint.h>
#include <type_traits>

typedef unsigned short u16;
typedef unsigned long long u64;
typedef __bf16 bf16x8 __attribute__((ext_vector_type(8)));
typedef __bf16 bf16x2 __attribute__((ext_vector_type(2)));
typedef short s16x2 __attribute__((ext_vector_type(2)));
typedef short s16x4 __attribute__((ext_vector_type(4)));
typedef float f32x2 __attribute__((ext_vector_type(2)));
typedef float f32x4 __attribute__((ext_vector_type(4)));
typedef float f32x16 __attribute__((ext_vector_type(16)));
typedef unsigned u32x2 __attribute__((ext_vector_type(2)));
typedef unsigned u32x4 __attribute__((ext_vector_type(4)));
typedef int i32x4 __attribute__((ext_vector_type(4)));
#define GAS __attribute__((address_space(1)))
#define LAS __attribute__((address_space(3)))
#define DI __device__ __forceinline__
#pragma clang diagnostic ignored "-Wint-to-pointer-cast"

constexpr int S_ = 2048, D_ = 448, F_ = 1792;
constexpr int NWG = 224;
constexpr unsigned FSTR = 256;
constexpr unsigned WS_EPOCH = 0;
constexpr unsigned WS_KVF = 4096;
constexpr unsigned WS_HF = WS_KVF + 1792 * FSTR;
constexpr unsigned WS_YF = WS_HF + 1792 * FSTR;
constexpr unsigned WS_W2F = WS_YF + 1792 * FSTR;
constexpr unsigned WS_STG = WS_W2F + 1792 * FSTR;
constexpr unsigned WS_CSG = WS_STG + 2048 * 8;
constexpr unsigned WS_KVT = 2u << 20;
constexpr unsigned WS_YT = 4u << 20;
constexpr unsigned WS_HT = 6u << 20;
constexpr unsigned WS_W2T = WS_HT + 64u * 112u * 1024u;
constexpr unsigned WS_END = WS_W2T + 7u * 112u * 2048u;
static_assert(WS_CSG + 1792 * 8 <= WS_KVT, "ws");
constexpr unsigned WS_DBG = 16u << 20;
constexpr unsigned L_X = 49152;
constexpr unsigned L_W = 0;
constexpr unsigned L_E = 98304;
constexpr unsigned E_SZ = 16640;
constexpr unsigned U_PL = 1568, U_SZ = 4 * U_PL;
constexpr unsigned L_SH = L_E + 2 * E_SZ;
constexpr unsigned L_SS = L_SH + 8192 + 64;
constexpr unsigned L_KV = L_SS + 1024;
constexpr unsigned L_Y = L_KV;
constexpr unsigned L_ST = L_KV + 9216;
constexpr unsigned L_U = L_ST + 1024;
constexpr unsigned P_SZ = 8704;
constexpr unsigned L_CP = 0;
constexpr unsigned L_G0 = 69632;
constexpr unsigned G_SZ = 8448;
constexpr unsigned L_G1 = 0;
constexpr unsigned L_SK = L_U;
constexpr unsigned L_TP = L_U + 9216;
constexpr unsigned L_SV = L_KV;
constexpr unsigned L_KO = L_KV;
constexpr unsigned L_SW = L_SH;
constexpr unsigned G_H = 528, G_G = 1056, G_T = 4224;
constexpr unsigned LDS_BYTES = L_U + 2 * U_SZ;
static_assert(L_G0 + 8 * G_SZ <= L_KV && L_U % 16 == 0, "lds");
static_assert(LDS_BYTES <= 163840, "lds");

constexpr unsigned SPIN_CAP = 1u << 18;
constexpr unsigned ONES = 0x3f803f80u, ONE_HI = 0x3f800000u;

DI float bflo(unsigned v) { return __uint_as_float(v << 16); }
DI float bfhi(unsigned v) { return __uint_as_float(v & 0xffff0000u); }
DI unsigned pk(float lo, float hi) { f32x2 f = {lo, hi}; return __builtin_bit_cast(unsigned, __builtin_convertvector(f, bf16x2)); }
DI float fmul1(float a, float b) { float d; asm("v_mul_f32 %0, %1, %2" : "=v"(d) : "v"(a), "v"(b)); return d; }
DI float rbf(float a) { return bfhi(pk(a, a)); }
DI float dot2(unsigned a, unsigned b, float c) { return __builtin_amdgcn_fdot2_f32_bf16(__builtin_bit_cast(bf16x2, a), __builtin_bit_cast(bf16x2, b), c, false); }
DI void dot2x8(float (&d)[8], const unsigned (&a)[8], unsigned b0, unsigned b1, unsigned b2, unsigned b3) {
  asm volatile("v_dot2_f32_bf16 %0, %8, %16, 0\n\tv_dot2_f32_bf16 %1, %9, %16, 0\n\tv_dot2_f32_bf16 %2, %10, %17, 0\n\tv_dot2_f32_bf16 %3, %11, %17, 0\n\t"
               "v_dot2_f32_bf16 %4, %12, %18, 0\n\tv_dot2_f32_bf16 %5, %13, %18, 0\n\tv_dot2_f32_bf16 %6, %14, %19, 0\n\tv_dot2_f32_bf16 %7, %15, %19, 0\n\ts_nop 2"
               : "=&v"(d[0]), "=&v"(d[1]), "=&v"(d[2]), "=&v"(d[3]), "=&v"(d[4]), "=&v"(d[5]), "=&v"(d[6]), "=&v"(d[7])
               : "v"(a[0]), "v"(a[1]), "v"(a[2]), "v"(a[3]), "v"(a[4]), "v"(a[5]), "v"(a[6]), "v"(a[7]), "v"(b0), "v"(b1), "v"(b2), "v"(b3));
}
DI f32x16 mfma(u32x4 a, u32x4 b, f32x16 c) { return __builtin_amdgcn_mfma_f32_32x32x16_bf16(__builtin_bit_cast(bf16x8, a), __builtin_bit_cast(bf16x8, b), c, 0, 0, 0); }
DI u32x4 lds128(unsigned a) { return *(const LAS u32x4*)a; }
DI u32x2 lds64(unsigned a) { return *(const LAS u32x2*)a; }
DI u32x2 ldstr(unsigned a) { return __builtin_bit_cast(u32x2, __builtin_amdgcn_ds_read_tr16_b64_v4i16((LAS s16x4*)a)); }
DI void sts128(unsigned a, u32x4 v) { *(LAS u32x4*)a = v; }
DI i32x4 mksrd(const void* p, unsigned bytes) {
  u64 a = (u64)p;
  i32x4 r = {(int)(unsigned)a, (int)((unsigned)(a >> 32) & 0xffffu), (int)bytes, 0x00020000};
  return r;
}
#define VMWAIT(n) asm volatile("s_waitcnt vmcnt(%0)" ::"i"(n) : "memory")
#define BAR() asm volatile("s_waitcnt lgkmcnt(0)\n\ts_barrier" ::: "memory")
#define OPAQUE(x) asm volatile("" : "+v"(x))
#define SB() __builtin_amdgcn_sched_barrier(0)
#define ROR(x, n) __uint_as_float(__builtin_amdgcn_update_dpp(0u, __float_as_uint(x), 0x120 + (n), 0xf, 0xf, false))
#define PIN8(a) asm volatile("" : "+v"(a[0]), "+v"(a[1]), "+v"(a[2]), "+v"(a[3]), "+v"(a[4]), "+v"(a[5]), "+v"(a[6]), "+v"(a[7]))
template <int LOFF, int GOFF> DI void dma(unsigned wb, unsigned voff, i32x4 srd, unsigned soff) {
  asm volatile("s_add_i32 m0, %0, %4\n\ts_nop 0\n\tbuffer_load_dwordx4 %1, %2, %3 offen offset:%5 lds"
               ::"s"(wb), "v"(voff), "s"(srd), "s"(soff), "i"(LOFF - GOFF), "i"(GOFF) : "scc");
}
template <typename R> DI void st128_sc1(u32x4 v, R rsrc, unsigned voff, unsigned soff) {
  __builtin_amdgcn_raw_buffer_store_b128(v, rsrc, voff, soff, 16);
  asm volatile("s_nop 1" ::"v"(v) : "memory");
}
DI void flag_store(char* ws, unsigned off, u64 tag, int lane) {
  if (lane == 0) __hip_atomic_store((GAS u64*)(ws + off), tag, __ATOMIC_RELAXED, __HIP_MEMORY_SCOPE_AGENT);
}
DI void flag_put(i32x4 srd, unsigned soff, u32x2 tagv) { asm volatile("buffer_store_dwordx2 %0, off, %1, %2 sc1" ::"v"(tagv), "s"(srd), "s"(soff) : "memory"); }
DI void poll_flags(char* ws, unsigned off, u64 tag) {
#pragma unroll 1
  for (unsigned spins = 0; spins < SPIN_CAP; ++spins) {
    u64 f = __hip_atomic_load((GAS u64*)(ws + off), __ATOMIC_RELAXED, __HIP_MEMORY_SCOPE_AGENT);
    if (__all(f == tag)) break;
    __builtin_amdgcn_s_sleep(1);
  }
  asm volatile("" ::: "memory");
}

DI unsigned poll_gran(char* ws, unsigned off, unsigned tag) {
  u64 f = 0;
#pragma unroll 1
  for (unsigned spins = 0; spins < SPIN_CAP; ++spins) {
    f = __hip_atomic_load((GAS u64*)(ws + off), __ATOMIC_RELAXED, __HIP_MEMORY_SCOPE_AGENT);
    if (__all((unsigned)(f >> 32) == tag)) break;
    __builtin_amdgcn_s_sleep(1);
  }
  asm volatile("" ::: "memory");
  return (unsigned)f;
}

#define STAMP(i) do {} while (0)
#define PART42 1
#define SIGF 7
#define HF0 3
#define NOW2 0
#define KSW 5
#define HPOLL 10
#define HCHK 13
#define HPS 3
#define TAUX 16
#define HAUX 16
#define ROT1 0
#define ROT2 0
#define ABL 0
#define YPOLL 17
#define YLAG 6
#define OUTAUX 16
#define HFIRST 6
#define HWIN 12

__global__ __launch_bounds__(512, 2) void fused_h3_mlp(const u16* __restrict__ gx, const u16* __restrict__ gk, const u16* __restrict__ gv,
                                                      const u16* __restrict__ gshk, const u16* __restrict__ gssk, const u16* __restrict__ gsD,
                                                      const u16* __restrict__ gw1, const u16* __restrict__ gb1, const u16* __restrict__ gw2,
                                                      const u16* __restrict__ gb2, u16* __restrict__ gout, char* __restrict__ ws) {
  STAMP(0);
  const int g = blockIdx.x, yy = blockIdx.y, zz = blockIdx.z;
  const int w = __builtin_amdgcn_readfirstlane(threadIdx.x >> 6);
  const int lane = threadIdx.x & 63, r = lane & 31, hh = lane >> 5;
  const i32x4 sWS = mksrd(ws, WS_END);
  asm volatile("" ::"s"(gx), "s"(gk), "s"(gv), "s"(gshk), "s"(gssk), "s"(gsD), "s"(gw1), "s"(gb1), "s"(gw2), "s"(gb2), "s"(gout), "s"(ws));
  u64 tag;
  if (yy == 7) {
    const int a = g + 8 * zz;
    const i32x4 sX = mksrd(gx, S_ * D_ * 2), sW1 = mksrd(gw1, D_ * F_ * 2);
    u32x4 xr[8], wv[7];
    const int row0 = 256 * g + 64 * zz + 8 * w;
    const unsigned xo = lane < 56 && !(ABL & 4) ? lane * 16 : 0x7fffffffu;
#pragma unroll
    for (int j = 0; j < 8; ++j)
      asm volatile("buffer_load_dwordx4 %0, %1, %2, %3 offen" : "=v"(xr[j]) : "v"(xo), "s"(sX), "s"((row0 + j) * 896) : "memory");
    const int ci = (g >> 1) + 4 * zz, cb = 14 * (g & 1) + ci;
    const unsigned wo = ci < 14 && !(ABL & 4) ? (lane >> 3) * 3584 + (lane & 7) * 16 + cb * 128 : 0x7fffffffu;
#pragma unroll
    for (int j = 0; j < 7; ++j)
      asm volatile("buffer_load_dwordx4 %0, %1, %2, %3 offen" : "=v"(wv[j]) : "v"(wo), "s"(sW1), "s"((56 * w + 8 * j) * 3584) : "memory");
    asm volatile("s_load_dwordx2 %0, %1, 0x0\n\ts_waitcnt lgkmcnt(0)" : "=s"(tag) : "s"(ws) : "memory");
    tag += 1ull;
    {
      float v[16];
#pragma unroll
      for (int j = 0; j < 8; ++j) {
        asm volatile("s_waitcnt vmcnt(%1)" : "+v"(xr[j]) : "i"(14 - j) : "memory");
        float s = 0.f, q = 0.f;
#pragma unroll
        for (int e = 0; e < 4; ++e) { s = dot2(xr[j][e], ONES, s); q = dot2(xr[j][e], xr[j][e], q); }
        v[j] = s; v[8 + j] = q;
        asm volatile("" : "+v"(v[j]), "+v"(v[8 + j]));
      }
#pragma unroll
      for (int j = 0; j < 8; ++j) { auto t = __builtin_amdgcn_permlane32_swap(__float_as_uint(v[j]), __float_as_uint(v[8 + j]), false, false); v[j] = __uint_as_float(t[0]) + __uint_as_float(t[1]); }
#pragma unroll
      for (int j = 0; j < 4; ++j) { auto t = __builtin_amdgcn_permlane16_swap(__float_as_uint(v[j]), __float_as_uint(v[4 + j]), false, false); v[j] = __uint_as_float(t[0]) + __uint_as_float(t[1]); }
#pragma unroll
      for (int j = 0; j < 4; ++j) { v[j] += ROR(v[j], 8); v[j] += ROR(v[j], 4); v[j] += ROR(v[j], 2); v[j] += ROR(v[j], 1); }
      const float mine = (lane & 2) ? ((lane & 1) ? v[3] : v[2]) : ((lane & 1) ? v[1] : v[0]);
      const auto t = __builtin_amdgcn_permlane32_swap(__float_as_uint(mine), __float_as_uint(mine), false, false);
      const float sq = __uint_as_float(t[1]);
      const float mean = mine * (1.f / D_);
      const float var = fmaxf(sq * (1.f / D_) - mean * mean, 0.f);
      const float den = __builtin_amdgcn_sqrtf(rbf(rbf(var) + 1e-5f));
      if ((lane & 44) == 0)
        __hip_atomic_store((GAS u64*)(ws + WS_STG + (row0 + ((lane >> 4) & 1) * 4 + (lane & 3)) * 8), ((u64)(unsigned)tag << 32) | pk(mean, den), __ATOMIC_RELAXED, __HIP_MEMORY_SCOPE_AGENT);
    }
    {
      unsigned onelo = 0x3f80u; OPAQUE(onelo);
      float c[8];
#pragma unroll
      for (int e = 0; e < 8; ++e) c[e] = 0.f;
#pragma unroll
      for (int j = 0; j < 7; ++j) {
        asm volatile("s_waitcnt vmcnt(%1)" : "+v"(wv[j]) : "i"(7 - j) : "memory");
#pragma unroll
        for (int e = 0; e < 4; ++e) { c[2 * e] = dot2(wv[j][e], onelo, c[2 * e]); c[2 * e + 1] = dot2(wv[j][e], ONE_HI, c[2 * e + 1]); }
        PIN8(c);
      }
#pragma unroll
      for (int e = 0; e < 4; ++e) { auto t = __builtin_amdgcn_permlane32_swap(__float_as_uint(c[e]), __float_as_uint(c[4 + e]), false, false); c[e] = __uint_as_float(t[0]) + __uint_as_float(t[1]); }
#pragma unroll
      for (int e = 0; e < 2; ++e) { auto t = __builtin_amdgcn_permlane16_swap(__float_as_uint(c[e]), __float_as_uint(c[2 + e]), false, false); c[e] = __uint_as_float(t[0]) + __uint_as_float(t[1]); }
      c[0] += ROR(c[0], 8); c[1] += ROR(c[1], 8);
      *(LAS f32x2*)(w * 256 + (lane & 7) * 32 + (lane >> 4) * 8) = f32x2{c[0], c[1]};
      BAR();
      if (w == 0 && ci < 14) {
        float t = 0.f;
#pragma unroll
        for (int j = 0; j < 8; ++j) t += *(const LAS float*)(j * 256 + lane * 4);
        __hip_atomic_store((GAS u64*)(ws + WS_CSG + (64 * cb + lane) * 8), ((u64)(unsigned)tag << 32) | __float_as_uint(t), __ATOMIC_RELAXED, __HIP_MEMORY_SCOPE_AGENT);
      }
    }
    if (a == 31) {
#pragma unroll 1
      for (int j = 0; j < 4; ++j) poll_gran(ws, WS_STG + ((w * 4 + j) * 64 + lane) * 8, (unsigned)tag);
#pragma unroll 1
      for (unsigned spins = 0; spins < SPIN_CAP; ++spins) {
        bool ok = true;
#pragma unroll
        for (int j = 0; j < 4; ++j) {
          const unsigned i = (w * 4 + j) * 64 + lane;
          ok &= __hip_atomic_load((GAS u64*)(ws + WS_HF + (i < 1792 ? i : 0) * FSTR), __ATOMIC_RELAXED, __HIP_MEMORY_SCOPE_AGENT) == tag;
        }
        if (__all(ok)) break;
        __builtin_amdgcn_s_sleep(8);
      }
      BAR();
      if (w == 0) flag_store(ws, WS_EPOCH, tag, lane);
    }
    return;
  }
  const int mt128 = 4 * (g >> 1) + zz, ft = 7 * (g & 1) + yy, hsel = g & 1;
  const int mt64 = 2 * mt128 + hsel, nt = yy;
  const int fg = w & 3, tg = w >> 2;
  const int cg = yy, tt = g + 8 * zz;
  const int ch0 = 64 * cg + 2 * tt;

  const i32x4 sX = mksrd(gx, S_ * D_ * 2), sW1 = mksrd(gw1, D_ * F_ * 2);
  unsigned vx0, vx1, vw0, vw1;
  {
    const int row = 16 * w + (lane >> 3);
    vx0 = (128 * mt128 + row) * 896 + (((lane ^ (lane >> 4)) & 7) << 4);
    vx1 = (vx0 + 8 * 896) ^ 64;
    const int q = lane >> 4;
    vw0 = (8 * w + q) * 3584 + ft * 256 + (((lane & 15) ^ ((q << 2) | (2 * (w & 1)))) << 4);
    vw1 = (vw0 + 4 * 3584) ^ 16;
  }
  const unsigned wb = w * 2048, wb1 = w * 1024;
#define SLX(k) ((k) == 3 ? L_E + 16384 : L_X + ((k) & 3) * 16384)
#define SLW(k) ((k) == 3 ? L_E : L_W + ((k) & 3) * 16384)
  const int rot1 = ROT1 ? (yy + 2 * zz >= 7 ? yy + 2 * zz - 7 : yy + 2 * zz) : 0;
  auto kc = [&](int k) { const int q = k + rot1; return (unsigned)(q >= 7 ? q - 7 : q); };
#define DMA1(k, j) do { \
    if ((j) == 0) dma<SLX(k), 0>(wb, vx0, sX, kc(k) * 128); \
    if ((j) == 1) dma<SLW(k), 0>(wb, vw0, sW1, kc(k) * (64 * 3584)); \
    if ((j) == 2) dma<SLX(k) + 1024, 0>(wb, vx1, sX, kc(k) * 128); \
    if ((j) == 3) dma<SLW(k) + 1024, 0>(wb, vw1, sW1, kc(k) * (64 * 3584)); } while (0)
  DMA1(0, 0); DMA1(0, 1); DMA1(0, 2); DMA1(0, 3);
  const unsigned vr8 = (lane >> 3) * 896 + (lane & 7) * 16;
  dma<L_SW, 0>(wb1, vr8, mksrd(gw2, tt < 28 ? D_ * F_ * 2 : 0), (64 * tt + 8 * w) * 896 + cg * 128);
  __builtin_amdgcn_s_barrier();
  {
    const unsigned kvso = 64 * tt * 896 + cg * 128;
    const i32x4 sK = mksrd(gk, S_ * D_ * 2);
    if (w == 4) dma<L_SK, 0>(0, tt ? vr8 : 0x7fffffffu, sK, tt ? kvso - 8 * 896 : 0);
    if (w == 5) dma<L_TP, 0>(0, lane * 16, mksrd(gshk, D_ * 8), cg * 512);
    dma<L_SK + 1024, 0>(wb1, vr8, sK, kvso + 8 * w * 896);
    dma<L_SV, 0>(wb1, vr8, mksrd(gv, S_ * D_ * 2), kvso + 8 * w * 896);
  }
  DMA1(1, 0); DMA1(1, 1); DMA1(1, 2); DMA1(1, 3);
  const auto rWS = __builtin_amdgcn_make_buffer_rsrc(ws, 0, WS_END, 0x00020000);
  unsigned dpair;
  asm volatile("s_load_dwordx2 %0, %2, 0x0\n\ts_load_dword %1, %3, 0x0" : "=&s"(tag), "=&s"(dpair) : "s"(ws), "s"(gsD + ch0) : "memory");

  unsigned aA0, aA1, aB[4];
  const int blk = (lane >> 4) & 1, q4 = (lane & 15) >> 2, p4 = lane & 3;
  {
    const int ch = 4 * fg + 2 * blk + (p4 >> 1);
    aA0 = L_W + 256 * (8 * hh + q4) + 16 * (ch ^ ((q4 << 2) | (2 * hh))) + 8 * (p4 & 1);
    aA1 = (aA0 + 1024) ^ 16;
    const unsigned b0 = L_X + (64 * tg + r) * 128 + ((hh ^ ((r >> 1) & 7)) << 4);
#pragma unroll
    for (int j = 0; j < 4; ++j) { aB[j] = b0 ^ (j << 5); OPAQUE(aB[j]); }
    OPAQUE(aA0); OPAQUE(aA1);
  }
  f32x16 acc0, acc1;
#pragma unroll
  for (int i = 0; i < 16; ++i) { acc0[i] = 0.f; acc1[i] = 0.f; }
  u32x4 fA[2], fB0[2], fB1[2];
  auto ldf = [&](int n) {
    const int j = n & 3, k = n >> 2;
    unsigned pa0 = aA0, pa1 = aA1, pb = aB[j];
    if (k == 3) { pa0 += L_E - L_W; pa1 += L_E - L_W; pb += L_E + 16384 - L_X; OPAQUE(pa0); OPAQUE(pa1); OPAQUE(pb); }
    const unsigned so = k == 3 ? 0 : (k & 3) * 16384;
    u32x2 a0 = ldstr(pa0 + so + j * 4096), a1 = ldstr(pa1 + so + j * 4096);
    fA[n & 1] = u32x4{a0[0], a0[1], a1[0], a1[1]};
    fB0[n & 1] = lds128(pb + so); fB1[n & 1] = lds128(pb + so + 4096);
  };
#define STEP(n) do { \
    SB(); acc0 = mfma(fA[(n) & 1], fB0[(n) & 1], acc0); asm volatile("" : "+v"(acc0)); SB(); \
    if ((n) + 1 < 28) ldf((n) + 1); \
    SB(); acc1 = mfma(fA[(n) & 1], fB1[(n) & 1], acc1); asm volatile("" : "+v"(acc1)); SB(); \
  } while (0)
#define PIECE(m) DMA1(((m) >> 2) + 2, (m) & 3)
  unsigned kf_so = WS_KVF + ((cg * 32 + tt) * 8 + w) * FSTR, hf_so = WS_HF + ((2 * mt128 + tg) * 56 + 4 * ft + fg) * FSTR, yf_so = WS_YF + ((cg * 8 + w) * 32 + tt) * FSTR;
  i32x4 sH = mksrd(gssk, S_ * D_ * 2);
  unsigned h_so = ch0 * 4096 + wb1, w2t_so = tt < 28 ? WS_W2T + (cg * 112 + 4 * tt) * 2048 + w * 1024 : WS_END;
  asm volatile("" : "+s"(kf_so), "+s"(hf_so), "+s"(yf_so), "+s"(sH), "+s"(h_so), "+s"(w2t_so));
  unsigned kvf_off = WS_KVF + (cg * 256 + (8 * fg + (lane & 7)) * 8 + (tt >> 2)) * FSTR;
  unsigned w2f_off = WS_W2F + (cg * 256 + 28 * w + (lane < 28 ? lane : 0)) * FSTR;
  unsigned gs_off = WS_STG + (128 * mt128 + 64 * tg + lane) * 8, gc_off = WS_CSG + (128 * ft + 32 * fg + r) * 8, b1_off = (128 * ft + 32 * fg + r) * 2;
  unsigned ks_off = (64 * cg + 8 * w + (lane >> 3)) * 4096 + (lane & 7) * 16, ko_rd = L_KO + (8 * w + (lane >> 3)) * 128 + (((lane ^ (lane >> 3)) & 7) << 4);
  unsigned l16 = lane * 16;
  asm volatile("" : "+v"(kvf_off), "+v"(w2f_off), "+v"(gs_off), "+v"(gc_off), "+v"(b1_off), "+v"(ks_off), "+v"(ko_rd), "+v"(l16));
  STAMP(1);
  asm volatile("s_waitcnt vmcnt(6) lgkmcnt(0)\n\ts_barrier" : "+s"(tag), "+s"(dpair) :: "memory");
  tag += 1ull;
  u32x2 tagv = {(unsigned)tag, (unsigned)(tag >> 32)};
  OPAQUE(tagv);
  STAMP(2);
#define TRYF(SENT, SO, Y, FORCE) do { if (!(SENT)) { const unsigned ib = __builtin_amdgcn_s_getreg(7 | (31 << 11)); \
      if ((FORCE) || ((ib & 15u) | ((ib >> 18) & 48u)) <= (Y)) { VMWAIT(Y); flag_put(sWS, SO, tagv); SENT = true; } } } while (0)
  bool wsent = false, ksent = false;
  {
    ldf(0);
    const unsigned wa = L_SW + (16 * (w >> 1) + 4 * hh + q4) * 128 + (w & 1) * 64 + blk * 32 + p4 * 8;
    const u32x2 wa0 = ldstr(wa), wa1 = ldstr(wa + 1024);
    STEP(0);
    st128_sc1(u32x4{wa0[0], wa0[1], wa1[0], wa1[1]}, rWS, l16, w2t_so);
  }
  PIECE(0); STEP(1); PIECE(1); STEP(2); PIECE(2);
  VMWAIT(4); BAR();
  STAMP(3);
  {
    const unsigned ta = (8 * w + q4) * 128 + (lane >> 4) * 32 + p4 * 8;
    u32x2 k0 = ldstr(L_SK + 512 + ta), k1 = ldstr(L_SK + 1024 + ta), k2 = ldstr(L_SK + 1536 + ta);
    u32x2 v0 = ldstr(L_SV + ta), v1 = ldstr(L_SV + 512 + ta);
    u32x2 shk = lds64(L_TP + lane * 8);
    STEP(3);
    PIECE(3);
    BAR();
    PIECE(4);
    TRYF(wsent, kf_so + (WS_W2F - WS_KVF), 5, false);
    const float kr[11] = {bfhi(k0[0]), bflo(k0[1]), bfhi(k0[1]), bflo(k1[0]), bfhi(k1[0]), bflo(k1[1]), bfhi(k1[1]), bflo(k2[0]), bfhi(k2[0]), bflo(k2[1]), bfhi(k2[1])};
    const float tw0 = bflo(shk[0]), tw1 = bfhi(shk[0]), tw2 = bflo(shk[1]), tw3 = bfhi(shk[1]);
    float a[8]; unsigned pp[8];
#pragma unroll
    for (int j = 0; j < 8; ++j) pp[j] = pk(fmul1(kr[j], tw3), fmul1(kr[j + 1], tw2));
    PIECE(5);
    dot2x8(a, pp, ONES, ONES, ONES, ONES);
#pragma unroll
    for (int j = 0; j < 8; ++j) pp[j] = pk(a[j], fmul1(kr[j + 2], tw1));
    PIECE(6);
    dot2x8(a, pp, ONES, ONES, ONES, ONES);
#pragma unroll
    for (int j = 0; j < 8; ++j) pp[j] = pk(a[j], fmul1(kr[j + 3], tw0));
    PIECE(7);
    dot2x8(a, pp, ONES, ONES, ONES, ONES);
#pragma unroll
    for (int j = 0; j < 8; j += 2) { pp[j] = pk(a[j], 0.f); pp[j + 1] = pk(0.f, a[j + 1]); }
    dot2x8(a, pp, v0[0], v0[1], v1[0], v1[1]);
    sts128(L_KO + lane * 128 + ((w ^ (lane & 7)) << 4), u32x4{pk(a[7], a[6]), pk(a[5], a[4]), pk(a[3], a[2]), pk(a[1], a[0])});
    sts128(L_U + (fg >> 1) * U_SZ + ((fg & 1) * 2 + hh) * U_PL + r * 16, u32x4{0u, 0u, 0u, 0u});
    TRYF(wsent, kf_so + (WS_W2F - WS_KVF), 8, false);
    SB();
  }
  STAMP(20);
  STEP(4); PIECE(8); STEP(5); PIECE(9); STEP(6); PIECE(10);
  VMWAIT(7); TRYF(wsent, kf_so + (WS_W2F - WS_KVF), 11, true); BAR();
  STAMP(4);
  {
    const u32x4 o = lds128(ko_rd);
    STEP(7);
    st128_sc1(o, rWS, ks_off, WS_KVT + tt * 128);
  }
  PIECE(11); STEP(8); PIECE(12); STEP(9); PIECE(13); STEP(10); PIECE(14);
  VMWAIT(8); TRYF(ksent, kf_so, 4, false); BAR();
  STAMP(5);
  u32x2 pf, pw, gs, gc; unsigned b1v = 0; u32x4 uld;
  STEP(11); PIECE(15); STEP(12);
  TRYF(ksent, kf_so, 5, false);
  asm volatile("buffer_load_dwordx2 %0, %1, %2, 0 offen sc1" : "=v"(pw) : "v"(w2f_off), "s"(sWS) : "memory");
  PIECE(16); STEP(13); PIECE(17); STEP(14); PIECE(18);
  VMWAIT(8); TRYF(ksent, kf_so, 9, true);
  BAR();
  STAMP(6);
  u32x4 wf[28];
  const unsigned w2so = WS_W2T + (nt * 112 + 14 * w) * 2048;
  auto ldw2 = [&](int n0, int n1) {
#pragma unroll
    for (int n = n0; n < n1; ++n) { wf[n] = __builtin_amdgcn_raw_buffer_load_b128(rWS, l16 + (n & 3) * 1024, w2so + (n >> 2) * 4096, 16); SB(); }
  };
  STEP(15); PIECE(19);
  STEP(16); dma<L_SH, 0>(wb1, l16, sH, h_so);
  asm volatile("s_waitcnt vmcnt(5)" : "+v"(pw) :: "memory");
  if (!__all((((u64)pw[1] << 32) | pw[0]) == tag)) poll_flags(ws, w2f_off, tag);
  STEP(17); ldw2(0, 1);
  STEP(18); ldw2(1, 2);
  BAR();
  STAMP(7);
  STEP(19);
  asm volatile("global_load_short_d16_hi %0, %1, %2" : "+v"(b1v) : "v"(b1_off), "s"(gb1) : "memory");
  asm volatile("buffer_load_dwordx2 %0, %1, %2, 0 offen sc1" : "=v"(gs) : "v"(gs_off), "s"(sWS) : "memory");
  asm volatile("buffer_load_dwordx2 %0, %1, %2, 0 offen sc1" : "=v"(gc) : "v"(gc_off), "s"(sWS) : "memory");
  STEP(20); ldw2(2, 3); STEP(21); ldw2(3, 4); STEP(22); ldw2(4, 5);
  VMWAIT(8);
  BAR();
  STAMP(8);
  STEP(23); ldw2(5, 6);
  unsigned ed[4][3];
  {
    unsigned ra = L_SH + w * 1024 + lane * 4;
    OPAQUE(ra);
#pragma unroll
    for (int j = 0; j < 4; ++j)
#pragma unroll
      for (int e = 0; e < 3; ++e) ed[j][e] = *(const LAS unsigned*)(ra + j * 256 + e * 4);
  }
  STEP(24);
  asm volatile("buffer_load_dwordx2 %0, %1, %2, 0 offen sc1" : "=v"(pf) : "v"(kvf_off), "s"(sWS) : "memory");
  STEP(25); ldw2(6, 7);
  STEP(26); ldw2(7, 8);
  STEP(27);
  asm volatile("s_waitcnt vmcnt(7)" : "+v"(b1v), "+v"(gs), "+v"(gc) :: "memory");
  if (!__all(gs[1] == (unsigned)tag && gc[1] == (unsigned)tag)) { gs[0] = poll_gran(ws, gs_off, (unsigned)tag); gc[0] = poll_gran(ws, gc_off, (unsigned)tag); }
  STAMP(10);
  const unsigned u_so = WS_KVT + (ch0 + tg) * 4096 + fg * 1024;
  {
    if (fg == 0) *(LAS unsigned*)(L_ST + (64 * tg + lane) * 4) = gs[0];
    const unsigned nm = gs[0] ^ 0x8000u;
    const unsigned bm = __builtin_amdgcn_perm(nm, nm, 0x01000100u);
    const float c = __uint_as_float(gc[0]), chi = rbf(c), c1 = c - chi, cmid = rbf(c1);
    const u32x4 A = {pk(chi, cmid), pk(c1 - cmid, __uint_as_float(b1v)), 0u, 0u};
    acc0 = mfma(A, u32x4{hh ? 0u : bm, hh ? 0u : nm, 0u, 0u}, acc0);
    acc1 = mfma(A, u32x4{hh ? bm : 0u, hh ? nm : 0u, 0u, 0u}, acc1);
    asm volatile("s_waitcnt vmcnt(2)" : "+v"(pf) :: "memory");
    asm volatile("buffer_load_dwordx4 %0, %1, %2, %3 offen sc1" : "=v"(uld) : "v"(l16), "s"(sWS), "s"(u_so) : "memory");
    ldw2(8, 10);
    const unsigned so = WS_HT + ((4 * mt128 + 2 * tg) * 112 + 8 * ft + 2 * fg) * 1024;
    unsigned wa = L_E + 256 + tg * E_SZ + fg * 4096 + lane * 16;
    OPAQUE(wa);
#pragma unroll
    for (int q = 0; q < 4; ++q) {
      sts128(wa + q * 1024, u32x4{ed[q][0], ed[q][1], __builtin_amdgcn_alignbit(ed[q][1], ed[q][0], 16), __builtin_amdgcn_alignbit(ed[q][2], ed[q][1], 16)});
      u32x4 hv;
#pragma unroll
      for (int e = 0; e < 4; ++e) {
        float a0 = (q < 2 ? acc0 : acc1)[8 * (q & 1) + 2 * e], a1 = (q < 2 ? acc0 : acc1)[8 * (q & 1) + 2 * e + 1];
        a0 *= __builtin_fabsf(a0); OPAQUE(a0);
        a1 *= __builtin_fabsf(a1);
        const s16x2 m = __builtin_elementwise_max(__builtin_bit_cast(s16x2, pk(a0, a1)), s16x2{0, 0});
        hv[e] = __builtin_bit_cast(unsigned, m);
      }
      st128_sc1(hv, rWS, (ABL & 8) ? 0x7fffffffu : l16 + (q & 1) * 1024, so + (q >> 1) * 112 * 1024);
    }
  }
  STAMP(11);
  asm volatile("s_waitcnt vmcnt(6)" : "+v"(uld) :: "memory");
  if (!__all((((u64)pf[1] << 32) | pf[0]) == tag)) {
    poll_flags(ws, kvf_off, tag);
    asm volatile("buffer_load_dwordx4 %0, %1, %2, %3 offen sc1\n\ts_waitcnt vmcnt(0)" : "=&v"(uld) : "v"(l16), "s"(sWS), "s"(u_so) : "memory");
  }
  {
    const int n = 64 * fg + lane;
    sts128(L_U + tg * U_SZ + (n & 3) * U_PL + ((n >> 2) + 32) * 16, uld);
    if (fg == 0) {
      const unsigned h0 = __builtin_amdgcn_readfirstlane(ed[0][0]), h1 = __builtin_amdgcn_readfirstlane(__builtin_amdgcn_alignbit(ed[0][1], ed[0][0], 16));
      const int l = lane & 15;
      sts128(L_E + tg * E_SZ + l * 16, u32x4{0u, l == 15 ? h0 : 0u, l == 15 ? h0 << 16 : 0u, l == 15 ? h1 : l == 14 ? h0 << 16 : 0u});
    }
  }
  ldw2(10, 12);
  BAR();
  STAMP(21);
  const int tok = threadIdx.x >> 4, quad = threadIdx.x & 15;
  const unsigned hf_off = WS_HF + (mt64 * 56 + 7 * w + (lane < 7 ? lane : 6)) * FSTR;
  const unsigned xo = ((64 * mt64 + tok) * D_ + 64 * nt + 4 * quad) * 2;
  u32x2 hf, xr0, xr1, b2r;
  constexpr int WIN = HWIN, NH0 = (16 - HCHK) * HPS;
  u32x4 hb[WIN];
  const unsigned hso = WS_HT + (2 * mt64 * 112 + 14 * w) * 1024;
  auto ldh = [&](int n) {
    const int mt = n / 14, ks = n % 14;
    hb[n % WIN] = __builtin_amdgcn_raw_buffer_load_b128(rWS, l16, hso + mt * 112 * 1024 + ks * 1024, 16);
    SB();
  };
  {
    f32x16 c0, c1;
#pragma unroll
    for (int i = 0; i < 16; ++i) { c0[i] = 0.f; c1[i] = 0.f; }
    unsigned ea0 = L_E + tg * E_SZ + 8 * (32 + r - 8 * hh - 7 + 256 * fg - 16), ea1 = ea0 + 32, ea2 = ea0 + 8192, ea3 = ea0 + 8224;
    unsigned ua = L_U + tg * U_SZ + hh * U_PL + 16 * (r + 32 - 8 * fg - 7);
    OPAQUE(ea0); OPAQUE(ea1); OPAQUE(ea2); OPAQUE(ea3); OPAQUE(ua);
    u32x4 A0[2], A1[2], B0[2], B1[2];
    auto ldca = [&](int st) {
      const int dd = st >> 1, s = st & 1;
      const unsigned eo = 8 * (32 * dd + 16 * (1 - s)), uo = 2 * s * U_PL + 16 * (7 - dd);
      u32x2 x0 = lds64(ea0 + eo), x1 = lds64(ea1 + eo);
      B0[st & 1] = lds128(ua + uo);
      A0[st & 1] = u32x4{x0[0], x0[1], x1[0], x1[1]};
    };
    auto ldcb = [&](int st) {
      const int dd = st >> 1, s = st & 1;
      const unsigned eo = 8 * (32 * dd + 16 * (1 - s)), uo = 2 * s * U_PL + 16 * (7 - dd);
      B1[st & 1] = lds128(ua + uo + 512);
      u32x2 y0 = lds64(ea2 + eo), y1 = lds64(ea3 + eo);
      A1[st & 1] = u32x4{y0[0], y0[1], y1[0], y1[1]};
    };
    ldca(0); ldcb(0);
    SB();
    bool hsent = false;
#pragma unroll
    for (int st = 0; st < 16; ++st) {
      c0 = mfma(A0[st & 1], B0[st & 1], c0); asm volatile("" : "+v"(c0)); SB();
      if (st + 1 < 16) ldca(st + 1);
      SB();
      c1 = mfma(A0[st & 1], B1[st & 1], c1); asm volatile("" : "+v"(c1)); SB();
      if (st + 1 < 16) ldcb(st + 1);
      SB();
      c1 = mfma(A1[st & 1], B0[st & 1], c1); asm volatile("" : "+v"(c1)); SB();
      if (st >= HF0 && st <= SIGF && (st & 1) && !hsent) {
        const int younger = 2 + (st < 8 ? 2 * st : 16);
        const unsigned ib = __builtin_amdgcn_s_getreg(7 | (31 << 11));
        if (st == SIGF || ((ib & 15u) | ((ib >> 18) & 48u)) <= (unsigned)younger) {
          VMWAIT(younger);
          flag_put(sWS, hf_so, tagv);
          hsent = true;
        }
      }
      if (st < 8) ldw2(12 + 2 * st, 14 + 2 * st);
      if (st == 8) {
        asm volatile("buffer_load_dwordx2 %0, %1, %2, 0 offen" : "=v"(xr0) : "v"(xo), "s"(sX) : "memory");
        asm volatile("buffer_load_dwordx2 %0, %1, %2, %3 offen" : "=v"(xr1) : "v"(xo), "s"(sX), "s"(32 * 896) : "memory");
        asm volatile("global_load_dwordx2 %0, %1, %2" : "=v"(b2r) : "v"((64 * nt + 4 * quad) * 2), "s"(gb2) : "memory");
      }
      if (st == HPOLL) asm volatile("buffer_load_dwordx2 %0, %1, %2, 0 offen sc1" : "=v"(hf) : "v"(hf_off), "s"(sWS) : "memory");
      if (st == HCHK) {
        STAMP(9);
        asm volatile("s_waitcnt vmcnt(0)" : "+v"(hf), "+v"(xr0), "+v"(xr1), "+v"(b2r) :: "memory");
        if (!__all((((u64)hf[1] << 32) | hf[0]) == tag)) poll_flags(ws, hf_off, tag);
        STAMP(22);
      }
      if (st >= HCHK) {
#pragma unroll
        for (int n = (st - HCHK) * HPS; n < (st - HCHK + 1) * HPS; ++n) ldh(n);
      }
    }
    STAMP(12);
    const unsigned pb = L_CP + w * P_SZ + lane * 16;
#pragma unroll
    for (int g4 = 0; g4 < 4; ++g4) {
      *(LAS f32x4*)(pb + g4 * 1088) = f32x4{c0[4 * g4], c0[4 * g4 + 1], c0[4 * g4 + 2], c0[4 * g4 + 3]};
      *(LAS f32x4*)(pb + 4352 + g4 * 1088) = f32x4{c1[4 * g4], c1[4 * g4 + 1], c1[4 * g4 + 2], c1[4 * g4 + 3]};
    }
  }
  BAR();
  STAMP(13);
  {
    const int n = 32 * w + r, a = n >> 2, g4 = n & 3;
    const unsigned po = L_CP + hh * 4 * P_SZ + (a >> 5) * 4352 + g4 * 1088 + (a & 31) * 16;
    f32x4 lo = *(const LAS f32x4*)po, hi = *(const LAS f32x4*)(po + 512);
#pragma unroll
    for (int q = 1; q < 4; ++q) {
      lo += *(const LAS f32x4*)(po + q * P_SZ);
      hi += *(const LAS f32x4*)(po + q * P_SZ + 512);
    }
    const u32x4 kvc = lds128(L_U + hh * U_SZ + g4 * U_PL + (a + 32) * 16);
    const unsigned dlo = hh ? dpair >> 16 : dpair & 0xffffu, dhi = dlo << 16;
    float pd[8], yv[8];
    { const unsigned kk[8] = {kvc[3], kvc[2], kvc[1], kvc[0], kvc[3], kvc[2], kvc[1], kvc[0]}; dot2x8(pd, kk, dhi, dhi, dlo, dlo); }
    { const unsigned pp[8] = {pk(lo[0], pd[0]), pk(lo[1], pd[4]), pk(lo[2], pd[1]), pk(lo[3], pd[5]), pk(hi[0], pd[2]), pk(hi[1], pd[6]), pk(hi[2], pd[3]), pk(hi[3], pd[7])};
      dot2x8(yv, pp, ONES, ONES, ONES, ONES); }
    const u32x4 yo = {pk(yv[0], yv[1]), pk(yv[2], yv[3]), pk(yv[4], yv[5]), pk(yv[6], yv[7])};
    st128_sc1(yo, rWS, (ch0 + hh) * 4096 + n * 16, WS_YT);
  }
  STAMP(14);
#pragma unroll
  for (int n = NH0; n < WIN; ++n) ldh(n);
  auto gate = [&](unsigned ra, float r2, unsigned (&gt)[2]) {
    f32x4 sm = *(const LAS f32x4*)ra;
#pragma unroll
    for (int q = 1; q < 8; ++q) sm += *(const LAS f32x4*)(ra + q * G_SZ);
    const unsigned oa = pk(fmaf(sm[0], r2, bflo(b2r[0])), fmaf(sm[1], r2, bfhi(b2r[0]))), ob = pk(fmaf(sm[2], r2, bflo(b2r[1])), fmaf(sm[3], r2, bfhi(b2r[1])));
    const float o[4] = {bflo(oa), bfhi(oa), bflo(ob), bfhi(ob)};
    float sv[4];
#pragma unroll
    for (int e = 0; e < 4; ++e) sv[e] = o[e] * __builtin_amdgcn_rcpf(1.f + __builtin_amdgcn_exp2f(o[e] * -1.4426950408889634f));
    gt[0] = pk(sv[0], sv[1]); gt[1] = pk(sv[2], sv[3]);
  };
  auto xnorm = [&](u32x2 xr, unsigned st, unsigned (&xn)[2], float& r2, float* xf) {
    const float m = bflo(st), rd = __builtin_amdgcn_rcpf(bfhi(st));
    xf[0] = bflo(xr[0]); xf[1] = bfhi(xr[0]); xf[2] = bflo(xr[1]); xf[3] = bfhi(xr[1]);
    const unsigned da = pk(xf[0] - m, xf[1] - m), db = pk(xf[2] - m, xf[3] - m);
    xn[0] = pk(bflo(da) * rd, bfhi(da) * rd); xn[1] = pk(bflo(db) * rd, bfhi(db) * rd);
    r2 = rd * rd;
  };
  unsigned g0[2], g1[2], xn0[2], xn1[2];
  float r20, r21, xf[8];
  unsigned rp0 = L_G0 + quad * G_H + tok * 16, rp1 = rp0 - (L_G0 - L_G1), yra = L_Y + 4 * quad * 132 + tok * 2;
  const unsigned yf_off = WS_YF + ((nt * 8 + (mt64 >> 2)) * 32 + 4 * w + (lane & 3)) * FSTR;
  u32x2 yf; u32x4 yb;
  const unsigned yvo = (lane >> 3) * 4096 + (lane & 7) * 16, yso = WS_YT + (64 * nt + 8 * w) * 4096 + mt64 * 128;
  STAMP(15);
  {
    f32x16 ca[2], cb[2];
#pragma unroll
    for (int i = 0; i < 16; ++i) { ca[0][i] = 0.f; cb[0][i] = 0.f; ca[1][i] = 0.f; cb[1][i] = 0.f; }
    unsigned pb0 = L_G0 + w * G_SZ + hh * G_H + r * 16, pb1 = pb0 - (L_G0 - L_G1);
    OPAQUE(pb0); OPAQUE(pb1);
    auto wrp = [&](unsigned pb, const f32x16& c, int g4, int t) { *(LAS f32x4*)(pb + t * G_T + g4 * G_G) = f32x4{c[4 * g4], c[4 * g4 + 1], c[4 * g4 + 2], c[4 * g4 + 3]}; };
#pragma unroll
    for (int n = 0; n < 28; ++n) {
      const int mt = n / 14, ks = n % 14;
      if (n == NH0) { VMWAIT(WIN - 1); flag_put(sWS, yf_so, tagv); }
      ca[mt] = mfma(wf[2 * ks], hb[n % WIN], ca[mt]); asm volatile("" : "+v"(ca[mt])); SB();
      if (n >= 1 && n - 1 + WIN < 28) ldh(n - 1 + WIN);
      if (n >= 14 && n < 18) { wrp(pb0, ca[0], n - 14, 0); SB(); }
      cb[mt] = mfma(wf[2 * ks + 1], hb[n % WIN], cb[mt]); asm volatile("" : "+v"(cb[mt])); SB();
      if (n == 1) {
        const unsigned st0 = *(const LAS unsigned*)(L_ST + (64 * hsel + tok) * 4), st1 = *(const LAS unsigned*)(L_ST + (64 * hsel + 32 + tok) * 4);
        xnorm(xr0, st0, xn0, r20, xf); xnorm(xr1, st1, xn1, r21, xf + 4);
        asm volatile("" : "+v"(xn0[0]), "+v"(xn0[1]), "+v"(xn1[0]), "+v"(xn1[1]), "+v"(r20), "+v"(r21), "+v"(rp0), "+v"(rp1), "+v"(yra)); PIN8(xf); SB();
      }
      if (n >= 14 && n < 18) { wrp(pb0, cb[0], n - 14, 1); SB(); }
      if (n == 20) { BAR(); STAMP(16); }
      if (n == YPOLL) asm volatile("buffer_load_dwordx2 %0, %1, %2, 0 offen sc1" : "=v"(yf) : "v"(yf_off), "s"(sWS) : "memory");
      if (n == YPOLL + YLAG) {
        asm volatile("s_waitcnt vmcnt(0)" : "+v"(yf) :: "memory");
        asm volatile("buffer_load_dwordx4 %0, %1, %2, %3 offen sc1" : "=v"(yb) : "v"(yvo), "s"(sWS), "s"(yso) : "memory");
      }
    }
#pragma unroll
    for (int g4 = 0; g4 < 4; ++g4) { wrp(pb1, ca[1], g4, 0); wrp(pb1, cb[1], g4, 1); }
    SB();
    gate(rp0, r20, g0);
  }
  STAMP(17);
  asm volatile("s_waitcnt vmcnt(0)" : "+v"(yb) :: "memory");
  if (!__all((((u64)yf[1] << 32) | yf[0]) == tag)) {
    poll_flags(ws, yf_off, tag);
    asm volatile("buffer_load_dwordx4 %0, %1, %2, %3 offen sc1\n\ts_waitcnt vmcnt(0)" : "=&v"(yb) : "v"(yvo), "s"(sWS), "s"(yso) : "memory");
  }
  {
    const unsigned ya = L_Y + (8 * w + (lane >> 3)) * 132 + (lane & 7) * 16;
#pragma unroll
    for (int e = 0; e < 4; ++e) *(LAS unsigned*)(ya + 4 * e) = yb[e];
  }
  BAR();
  STAMP(18);
  {
    unsigned yv[8];
#pragma unroll
    for (int e = 0; e < 8; ++e) yv[e] = *(const LAS u16*)(yra + (e & 3) * 132 + (e >> 2) * 64);
    gate(rp1, r21, g1);
    float t[8]; unsigned pp[8];
#pragma unroll
    for (int e = 1; e < 8; e += 2) yv[e] <<= 16;
    dot2x8(t, yv, xn0[0], xn0[1], xn1[0], xn1[1]);
#pragma unroll
    for (int e = 0; e < 8; e += 2) { pp[e] = pk(t[e], 0.f); pp[e + 1] = pk(0.f, t[e + 1]); }
    dot2x8(t, pp, g0[0], g0[1], g1[0], g1[1]);
#pragma unroll
    for (int e = 0; e < 8; ++e) pp[e] = pk(t[e], xf[e]);
    dot2x8(t, pp, ONES, ONES, ONES, ONES);
    const auto rO = __builtin_amdgcn_make_buffer_rsrc(gout, 0, S_ * D_ * 2, 0x00020000);
    __builtin_amdgcn_raw_buffer_store_b64(u32x2{pk(t[0], t[1]), pk(t[2], t[3])}, rO, xo, 0, OUTAUX);
    __builtin_amdgcn_raw_buffer_store_b64(u32x2{pk(t[4], t[5]), pk(t[6], t[7])}, rO, xo, 32 * 896, OUTAUX);
  }
}

extern "C" void kernel_launch(void* const* d_in, const int* in_sizes, int n_in, void* d_out, int out_size, void* d_ws, size_t ws_size,
                              hipStream_t stream) {
  (void)in_sizes; (void)n_in; (void)out_size; (void)ws_size;
  (void)hipFuncSetAttribute((const void*)fused_h3_mlp, hipFuncAttributeMaxDynamicSharedMemorySize, LDS_BYTES);
  fused_h3_mlp<<<dim3(8, 8, 4), 512, LDS_BYTES, stream>>>((const u16*)d_in[0], (const u16*)d_in[1], (const u16*)d_in[2], (const u16*)d_in[3],
                                                         (const u16*)d_in[4], (const u16*)d_in[5], (const u16*)d_in[7], (const u16*)d_in[8],
                                                         (const u16*)d_in[9], (const u16*)d_in[10], (u16*)d_out, (char*)d_ws);
}
```

```cpp
#include <hip/hip_runtime.h>
#include <hip/hip_bf16.h>
#include <stdint.h>
#include <type_traits>

typedef unsigned short u16;
typedef unsigned long long u64;
typedef __bf16 bf16x8 __attribute__((ext_vector_type(8)));
typedef __bf16 bf16x2 __attribute__((ext_vector_type(2)));
typedef short s16x2 __attribute__((ext_vector_type(2)));
typedef short s16x4 __attribute__((ext_vector_type(4)));
typedef float f32x2 __attribute__((ext_vector_type(2)));
typedef float f32x4 __attribute__((ext_vector_type(4)));
typedef float f32x16 __attribute__((ext_vector_type(16)));
typedef unsigned u32x2 __attribute__((ext_vector_type(2)));
typedef unsigned u32x4 __attribute__((ext_vector_type(4)));
typedef int i32x4 __attribute__((ext_vector_type(4)));
#define GAS __attribute__((address_space(1)))
#define LAS __attribute__((address_space(3)))
#define DI __device__ __forceinline__
#pragma clang diagnostic ignored "-Wint-to-pointer-cast"

constexpr int S_ = 2048, D_ = 448, F_ = 1792;
constexpr unsigned FSTR = 256;
constexpr unsigned WS_EPOCH = 0;
constexpr unsigned WS_KVF = 4096;
constexpr unsigned WS_HF = WS_KVF + 1792 * FSTR;
constexpr unsigned WS_YF = WS_HF + 1792 * FSTR;
constexpr unsigned WS_W2F = WS_YF + 1792 * FSTR;
constexpr unsigned WS_STG = WS_W2F + 1792 * FSTR;
constexpr unsigned WS_CSG = WS_STG + 2048 * 8;
constexpr unsigned WS_KVT = 2u << 20;
constexpr unsigned WS_YT = 4u << 20;
constexpr unsigned WS_HT = 6u << 20;
constexpr unsigned WS_W2T = WS_HT + 64u * 112u * 1024u;
constexpr unsigned WS_END = WS_W2T + 7u * 112u * 2048u;
static_assert(WS_CSG + 1792 * 8 <= WS_KVT, "ws");
constexpr unsigned L_X = 49152;
constexpr unsigned L_W = 0;
constexpr unsigned L_E = 98304;
constexpr unsigned E_SZ = 16640;
constexpr unsigned U_PL = 1568, U_SZ = 4 * U_PL;
constexpr unsigned L_SH = L_E + 2 * E_SZ;
constexpr unsigned L_SS = L_SH + 8192 + 64;
constexpr unsigned L_KV = L_SS + 1024;
constexpr unsigned L_Y = L_KV;
constexpr unsigned L_ST = L_KV + 9216;
constexpr unsigned L_U = L_ST + 1024;
constexpr unsigned P_SZ = 8704;
constexpr unsigned L_CP = 0;
constexpr unsigned L_G0 = 69632;
constexpr unsigned G_SZ = 8448;
constexpr unsigned L_G1 = 0;
constexpr unsigned L_SK = L_U;
constexpr unsigned L_TP = L_U + 9216;
constexpr unsigned L_SV = L_KV;
constexpr unsigned L_KO = L_KV;
constexpr unsigned L_SW = L_SH;
constexpr unsigned G_H = 528, G_G = 1056, G_T = 4224;
constexpr unsigned LDS_BYTES = L_U + 2 * U_SZ;
static_assert(L_G0 + 8 * G_SZ <= L_KV && L_U % 16 == 0, "lds");
static_assert(LDS_BYTES <= 163840, "lds");

constexpr unsigned SPIN_CAP = 1u << 18;
constexpr unsigned ONES = 0x3f803f80u, ONE_HI = 0x3f800000u;

DI float bflo(unsigned v) { return __uint_as_float(v << 16); }
DI float bfhi(unsigned v) { return __uint_as_float(v & 0xffff0000u); }
DI unsigned pk(float lo, float hi) { f32x2 f = {lo, hi}; return __builtin_bit_cast(unsigned, __builtin_convertvector(f, bf16x2)); }
DI float fmul1(float a, float b) { float d; asm("v_mul_f32 %0, %1, %2" : "=v"(d) : "v"(a), "v"(b)); return d; }
DI float fadd1(float a, float b) { float d; asm("v_add_f32 %0, %1, %2" : "=v"(d) : "v"(a), "v"(b)); return d; }
DI float ffma1(float a, float b, float c) { float d; asm("v_fma_f32 %0, %1, %2, %3" : "=v"(d) : "v"(a), "v"(b), "v"(c)); return d; }
DI float rbf(float a) { return bfhi(pk(a, a)); }
DI float dot2(unsigned a, unsigned b, float c) { return __builtin_amdgcn_fdot2_f32_bf16(__builtin_bit_cast(bf16x2, a), __builtin_bit_cast(bf16x2, b), c, false); }
DI void dot2x8(float (&d)[8], const unsigned (&a)[8], unsigned b0, unsigned b1, unsigned b2, unsigned b3) {
  asm volatile("v_dot2_f32_bf16 %0, %8, %16, 0\n\tv_dot2_f32_bf16 %1, %9, %16, 0\n\tv_dot2_f32_bf16 %2, %10, %17, 0\n\tv_dot2_f32_bf16 %3, %11, %17, 0\n\t"
               "v_dot2_f32_bf16 %4, %12, %18, 0\n\tv_dot2_f32_bf16 %5, %13, %18, 0\n\tv_dot2_f32_bf16 %6, %14, %19, 0\n\tv_dot2_f32_bf16 %7, %15, %19, 0\n\ts_nop 2"
               : "=&v"(d[0]), "=&v"(d[1]), "=&v"(d[2]), "=&v"(d[3]), "=&v"(d[4]), "=&v"(d[5]), "=&v"(d[6]), "=&v"(d[7])
               : "v"(a[0]), "v"(a[1]), "v"(a[2]), "v"(a[3]), "v"(a[4]), "v"(a[5]), "v"(a[6]), "v"(a[7]), "v"(b0), "v"(b1), "v"(b2), "v"(b3));
}
DI f32x16 mfma(u32x4 a, u32x4 b, f32x16 c) { return __builtin_amdgcn_mfma_f32_32x32x16_bf16(__builtin_bit_cast(bf16x8, a), __builtin_bit_cast(bf16x8, b), c, 0, 0, 0); }
DI u32x4 lds128(unsigned a) { return *(const LAS u32x4*)a; }
DI u32x2 lds64(unsigned a) { return *(const LAS u32x2*)a; }
DI u32x2 ldstr(unsigned a) { return __builtin_bit_cast(u32x2, __builtin_amdgcn_ds_read_tr16_b64_v4i16((LAS s16x4*)a)); }
DI void sts128(unsigned a, u32x4 v) { *(LAS u32x4*)a = v; }
DI i32x4 mksrd(const void* p, unsigned bytes) {
  u64 a = (u64)p;
  i32x4 r = {(int)(unsigned)a, (int)((unsigned)(a >> 32) & 0xffffu), (int)bytes, 0x00020000};
  return r;
}
#define VMWAIT(n) asm volatile("s_waitcnt vmcnt(%0)" ::"i"(n) : "memory")
#define BAR() asm volatile("s_waitcnt lgkmcnt(0)\n\ts_barrier" ::: "memory")
#define OPAQUE(x) asm volatile("" : "+v"(x))
#define SB() __builtin_amdgcn_sched_barrier(0)
#define ROR(x, n) __uint_as_float(__builtin_amdgcn_update_dpp(0u, __float_as_uint(x), 0x120 + (n), 0xf, 0xf, false))
#define PIN8(a) asm volatile("" : "+v"(a[0]), "+v"(a[1]), "+v"(a[2]), "+v"(a[3]), "+v"(a[4]), "+v"(a[5]), "+v"(a[6]), "+v"(a[7]))
template <int LOFF, int GOFF> DI void dma(unsigned wb, unsigned voff, i32x4 srd, unsigned soff) {
  asm volatile("s_add_i32 m0, %0, %4\n\ts_nop 0\n\tbuffer_load_dwordx4 %1, %2, %3 offen offset:%5 lds"
               ::"s"(wb), "v"(voff), "s"(srd), "s"(soff), "i"(LOFF - GOFF), "i"(GOFF) : "scc");
}
template <typename R> DI void st128_sc1(u32x4 v, R rsrc, unsigned voff, unsigned soff) {
  __builtin_amdgcn_raw_buffer_store_b128(v, rsrc, voff, soff, 16);
  asm volatile("s_nop 1" ::"v"(v) : "memory");
}
DI void flag_store(char* ws, unsigned off, u64 tag, int lane) {
  if (lane == 0) __hip_atomic_store((GAS u64*)(ws + off), tag, __ATOMIC_RELAXED, __HIP_MEMORY_SCOPE_AGENT);
}
DI void flag_put(i32x4 srd, unsigned soff, u32x2 tagv) { asm volatile("buffer_store_dwordx2 %0, off, %1, %2 sc1" ::"v"(tagv), "s"(srd), "s"(soff) : "memory"); }
DI void poll_flags(char* ws, unsigned off, u64 tag) {
#pragma unroll 1
  for (unsigned spins = 0; spins < SPIN_CAP; ++spins) {
    u64 f = __hip_atomic_load((GAS u64*)(ws + off), __ATOMIC_RELAXED, __HIP_MEMORY_SCOPE_AGENT);
    if (__all(f == tag)) break;
    __builtin_amdgcn_s_sleep(1);
  }
  asm volatile("" ::: "memory");
}

DI unsigned poll_gran(char* ws, unsigned off, unsigned tag) {
  u64 f = 0;
#pragma unroll 1
  for (unsigned spins = 0; spins < SPIN_CAP; ++spins) {
    f = __hip_atomic_load((GAS u64*)(ws + off), __ATOMIC_RELAXED, __HIP_MEMORY_SCOPE_AGENT);
    if (__all((unsigned)(f >> 32) == tag)) break;
    __builtin_amdgcn_s_sleep(1);
  }
  asm volatile("" ::: "memory");
  return (unsigned)f;
}

constexpr int HF0 = 3, SIGF = 7;
constexpr int HPOLL = 10, HCHK = 13, HPS = 3;
constexpr int YPOLL = 15, YLAG = 6;
constexpr int HWIN = 12;

__global__ __launch_bounds__(512, 2) void fused_h3_mlp(const u16* __restrict__ gx, const u16* __restrict__ gk, const u16* __restrict__ gv,
                                                      const u16* __restrict__ gshk, const u16* __restrict__ gssk, const u16* __restrict__ gsD,
                                                      const u16* __restrict__ gw1, const u16* __restrict__ gb1, const u16* __restrict__ gw2,
                                                      const u16* __restrict__ gb2, u16* __restrict__ gout, char* __restrict__ ws) {
  const int g = blockIdx.x, yy = blockIdx.y, zz = blockIdx.z;
  const int w = __builtin_amdgcn_readfirstlane(threadIdx.x >> 6);
  const int lane = threadIdx.x & 63, r = lane & 31, hh = lane >> 5;
  const i32x4 sWS = mksrd(ws, WS_END);
  asm volatile("" ::"s"(gx), "s"(gk), "s"(gv), "s"(gshk), "s"(gssk), "s"(gsD), "s"(gw1), "s"(gb1), "s"(gw2), "s"(gb2), "s"(gout), "s"(ws));
  u64 tag;
  if (yy == 7) {
    const int a = g + 8 * zz;
    const i32x4 sX = mksrd(gx, S_ * D_ * 2), sW1 = mksrd(gw1, D_ * F_ * 2);
    u32x4 xr[8], wv[7];
    const int row0 = 256 * g + 64 * zz + 8 * w;
    const unsigned xo = lane < 56 ? lane * 16 : 0x7fffffffu;
#pragma unroll
    for (int j = 0; j < 8; ++j)
      asm volatile("buffer_load_dwordx4 %0, %1, %2, %3 offen" : "=v"(xr[j]) : "v"(xo), "s"(sX), "s"((row0 + j) * 896) : "memory");
    const int ci = (g >> 1) + 4 * zz, cb = 14 * (g & 1) + ci;
    const unsigned wo = ci < 14 ? (lane >> 3) * 3584 + (lane & 7) * 16 + cb * 128 : 0x7fffffffu;
#pragma unroll
    for (int j = 0; j < 7; ++j)
      asm volatile("buffer_load_dwordx4 %0, %1, %2, %3 offen" : "=v"(wv[j]) : "v"(wo), "s"(sW1), "s"((56 * w + 8 * j) * 3584) : "memory");
    asm volatile("s_load_dwordx2 %0, %1, 0x0\n\ts_waitcnt lgkmcnt(0)" : "=s"(tag) : "s"(ws) : "memory");
    tag += 1ull;
    {
      float v[16];
#pragma unroll
      for (int j = 0; j < 8; ++j) {
        asm volatile("s_waitcnt vmcnt(%1)" : "+v"(xr[j]) : "i"(14 - j) : "memory");
        float s = 0.f, q = 0.f;
#pragma unroll
        for (int e = 0; e < 4; ++e) { s = dot2(xr[j][e], ONES, s); q = dot2(xr[j][e], xr[j][e], q); }
        v[j] = s; v[8 + j] = q;
        asm volatile("" : "+v"(v[j]), "+v"(v[8 + j]));
      }
#pragma unroll
      for (int j = 0; j < 8; ++j) { auto t = __builtin_amdgcn_permlane32_swap(__float_as_uint(v[j]), __float_as_uint(v[8 + j]), false, false); v[j] = __uint_as_float(t[0]) + __uint_as_float(t[1]); }
#pragma unroll
      for (int j = 0; j < 4; ++j) { auto t = __builtin_amdgcn_permlane16_swap(__float_as_uint(v[j]), __float_as_uint(v[4 + j]), false, false); v[j] = __uint_as_float(t[0]) + __uint_as_float(t[1]); }
#pragma unroll
      for (int j = 0; j < 4; ++j) { v[j] += ROR(v[j], 8); v[j] += ROR(v[j], 4); v[j] += ROR(v[j], 2); v[j] += ROR(v[j], 1); }
      const float mine = (lane & 2) ? ((lane & 1) ? v[3] : v[2]) : ((lane & 1) ? v[1] : v[0]);
      const auto t = __builtin_amdgcn_permlane32_swap(__float_as_uint(mine), __float_as_uint(mine), false, false);
      const float sq = __uint_as_float(t[1]);
      const float mean = mine * (1.f / D_);
      const float var = fmaxf(sq * (1.f / D_) - mean * mean, 0.f);
      const float den = __builtin_amdgcn_sqrtf(rbf(rbf(var) + 1e-5f));
      if ((lane & 44) == 0)
        __hip_atomic_store((GAS u64*)(ws + WS_STG + (row0 + ((lane >> 4) & 1) * 4 + (lane & 3)) * 8), ((u64)(unsigned)tag << 32) | pk(mean, den), __ATOMIC_RELAXED, __HIP_MEMORY_SCOPE_AGENT);
    }
    {
      unsigned onelo = 0x3f80u; OPAQUE(onelo);
      float c[8];
#pragma unroll
      for (int e = 0; e < 8; ++e) c[e] = 0.f;
#pragma unroll
      for (int j = 0; j < 7; ++j) {
        asm volatile("s_waitcnt vmcnt(%1)" : "+v"(wv[j]) : "i"(7 - j) : "memory");
#pragma unroll
        for (int e = 0; e < 4; ++e) { c[2 * e] = dot2(wv[j][e], onelo, c[2 * e]); c[2 * e + 1] = dot2(wv[j][e], ONE_HI, c[2 * e + 1]); }
        PIN8(c);
      }
#pragma unroll
      for (int e = 0; e < 4; ++e) { auto t = __builtin_amdgcn_permlane32_swap(__float_as_uint(c[e]), __float_as_uint(c[4 + e]), false, false); c[e] = __uint_as_float(t[0]) + __uint_as_float(t[1]); }
#pragma unroll
      for (int e = 0; e < 2; ++e) { auto t = __builtin_amdgcn_permlane16_swap(__float_as_uint(c[e]), __float_as_uint(c[2 + e]), false, false); c[e] = __uint_as_float(t[0]) + __uint_as_float(t[1]); }
      c[0] += ROR(c[0], 8); c[1] += ROR(c[1], 8);
      *(LAS f32x2*)(w * 256 + (lane & 7) * 32 + (lane >> 4) * 8) = f32x2{c[0], c[1]};
      BAR();
      if (w == 0 && ci < 14) {
        float t = 0.f;
#pragma unroll
        for (int j = 0; j < 8; ++j) t += *(const LAS float*)(j * 256 + lane * 4);
        __hip_atomic_store((GAS u64*)(ws + WS_CSG + (64 * cb + lane) * 8), ((u64)(unsigned)tag << 32) | __float_as_uint(t), __ATOMIC_RELAXED, __HIP_MEMORY_SCOPE_AGENT);
      }
    }
    if (a == 31) {
#pragma unroll 1
      for (int j = 0; j < 4; ++j) poll_gran(ws, WS_STG + ((w * 4 + j) * 64 + lane) * 8, (unsigned)tag);
#pragma unroll 1
      for (unsigned spins = 0; spins < SPIN_CAP; ++spins) {
        bool ok = true;
#pragma unroll
        for (int j = 0; j < 4; ++j) {
          const unsigned i = (w * 4 + j) * 64 + lane;
          ok &= __hip_atomic_load((GAS u64*)(ws + WS_HF + (i < 1792 ? i : 0) * FSTR), __ATOMIC_RELAXED, __HIP_MEMORY_SCOPE_AGENT) == tag;
        }
        if (__all(ok)) break;
        __builtin_amdgcn_s_sleep(8);
      }
      BAR();
      if (w == 0) flag_store(ws, WS_EPOCH, tag, lane);
    }
    return;
  }
  const int mt128 = 4 * (g >> 1) + zz, ft = 7 * (g & 1) + yy, hsel = g & 1;
  const int mt64 = 2 * mt128 + hsel, nt = yy;
  const int fg = w & 3, tg = w >> 2;
  const int cg = yy, tt = g + 8 * zz;
  const int ch0 = 64 * cg + 2 * tt;

  const i32x4 sX = mksrd(gx, S_ * D_ * 2), sW1 = mksrd(gw1, D_ * F_ * 2);
  unsigned vx0, vx1, vw0, vw1;
  {
    const int row = 16 * w + (lane >> 3);
    vx0 = (128 * mt128 + row) * 896 + (((lane ^ (lane >> 4)) & 7) << 4);
    vx1 = (vx0 + 8 * 896) ^ 64;
    const int q = lane >> 4;
    vw0 = (8 * w + q) * 3584 + ft * 256 + (((lane & 15) ^ ((q << 2) | (2 * (w & 1)))) << 4);
    vw1 = (vw0 + 4 * 3584) ^ 16;
  }
  const unsigned wb = w * 2048, wb1 = w * 1024;
#define SLX(k) ((k) == 3 ? L_E + 16384 : L_X + ((k) & 3) * 16384)
#define SLW(k) ((k) == 3 ? L_E : L_W + ((k) & 3) * 16384)
#define DMA1(k, j) do { \
    if ((j) == 0) dma<SLX(k), (k) * 128>(wb, vx0, sX, 0); \
    if ((j) == 1) dma<SLW(k), 0>(wb, vw0, sW1, (k) * 64 * 3584); \
    if ((j) == 2) dma<SLX(k) + 1024, (k) * 128>(wb, vx1, sX, 0); \
    if ((j) == 3) dma<SLW(k) + 1024, 0>(wb, vw1, sW1, (k) * 64 * 3584); } while (0)
  DMA1(0, 0); DMA1(0, 1); DMA1(0, 2); DMA1(0, 3);
  const unsigned vr8 = (lane >> 3) * 896 + (lane & 7) * 16;
  dma<L_SW, 0>(wb1, vr8, mksrd(gw2, tt < 28 ? D_ * F_ * 2 : 0), (64 * tt + 8 * w) * 896 + cg * 128);
  __builtin_amdgcn_s_barrier();
  {
    const unsigned kvso = 64 * tt * 896 + cg * 128;
    const i32x4 sK = mksrd(gk, S_ * D_ * 2);
    if (w == 4) dma<L_SK, 0>(0, tt ? vr8 : 0x7fffffffu, sK, tt ? kvso - 8 * 896 : 0);
    if (w == 5) dma<L_TP, 0>(0, lane * 16, mksrd(gshk, D_ * 8), cg * 512);
    dma<L_SK + 1024, 0>(wb1, vr8, sK, kvso + 8 * w * 896);
    dma<L_SV, 0>(wb1, vr8, mksrd(gv, S_ * D_ * 2), kvso + 8 * w * 896);
  }
  DMA1(1, 0); DMA1(1, 1); DMA1(1, 2); DMA1(1, 3);
  const auto rWS = __builtin_amdgcn_make_buffer_rsrc(ws, 0, WS_END, 0x00020000);
  unsigned dpair;
  asm volatile("s_load_dwordx2 %0, %2, 0x0\n\ts_load_dword %1, %3, 0x0" : "=&s"(tag), "=&s"(dpair) : "s"(ws), "s"(gsD + ch0) : "memory");

  unsigned aA0, aA1, aB[4];
  const int blk = (lane >> 4) & 1, q4 = (lane & 15) >> 2, p4 = lane & 3;
  {
    const int ch = 4 * fg + 2 * blk + (p4 >> 1);
    aA0 = L_W + 256 * (8 * hh + q4) + 16 * (ch ^ ((q4 << 2) | (2 * hh))) + 8 * (p4 & 1);
    aA1 = (aA0 + 1024) ^ 16;
    const unsigned b0 = L_X + (64 * tg + r) * 128 + ((hh ^ ((r >> 1) & 7)) << 4);
#pragma unroll
    for (int j = 0; j < 4; ++j) { aB[j] = b0 ^ (j << 5); OPAQUE(aB[j]); }
    OPAQUE(aA0); OPAQUE(aA1);
  }
  f32x16 acc0, acc1;
#pragma unroll
  for (int i = 0; i < 16; ++i) { acc0[i] = 0.f; acc1[i] = 0.f; }
  u32x4 fA[2], fB0[2], fB1[2];
  auto ldf = [&](int n) {
    const int j = n & 3, k = n >> 2;
    unsigned pa0 = aA0, pa1 = aA1, pb = aB[j];
    if (k == 3) { pa0 += L_E - L_W; pa1 += L_E - L_W; pb += L_E + 16384 - L_X; OPAQUE(pa0); OPAQUE(pa1); OPAQUE(pb); }
    const unsigned so = k == 3 ? 0 : (k & 3) * 16384;
    u32x2 a0 = ldstr(pa0 + so + j * 4096), a1 = ldstr(pa1 + so + j * 4096);
    fA[n & 1] = u32x4{a0[0], a0[1], a1[0], a1[1]};
    fB0[n & 1] = lds128(pb + so); fB1[n & 1] = lds128(pb + so + 4096);
  };
#define STEP(n) do { \
    SB(); acc0 = mfma(fA[(n) & 1], fB0[(n) & 1], acc0); asm volatile("" : "+v"(acc0)); SB(); \
    if ((n) + 1 < 28) ldf((n) + 1); \
    SB(); acc1 = mfma(fA[(n) & 1], fB1[(n) & 1], acc1); asm volatile("" : "+v"(acc1)); SB(); \
  } while (0)
#define PIECE(m) DMA1(((m) >> 2) + 2, (m) & 3)
  unsigned kf_so = WS_KVF + ((cg * 32 + tt) * 8 + w) * FSTR, hf_so = WS_HF + ((2 * mt128 + tg) * 56 + 4 * ft + fg) * FSTR, yf_so = WS_YF + ((cg * 8 + w) * 32 + tt) * FSTR;
  i32x4 sH = mksrd(gssk, S_ * D_ * 2);
  unsigned h_so = ch0 * 4096 + wb1, w2t_so = tt < 28 ? WS_W2T + (cg * 112 + 4 * tt) * 2048 + w * 1024 : WS_END;
  asm volatile("" : "+s"(kf_so), "+s"(hf_so), "+s"(yf_so), "+s"(sH), "+s"(h_so), "+s"(w2t_so));
  unsigned kvf_off = WS_KVF + (cg * 256 + (8 * fg + (lane & 7)) * 8 + (tt >> 2)) * FSTR;
  unsigned w2f_off = WS_W2F + (cg * 256 + 28 * w + (lane < 28 ? lane : 0)) * FSTR;
  unsigned gs_off = WS_STG + (128 * mt128 + 64 * tg + lane) * 8, gc_off = WS_CSG + (128 * ft + 32 * fg + r) * 8, b1_off = (128 * ft + 32 * fg + r) * 2;
  unsigned ks_off = (64 * cg + 8 * w + (lane >> 3)) * 4096 + (lane & 7) * 16, ko_rd = L_KO + (8 * w + (lane >> 3)) * 128 + (((lane ^ (lane >> 3)) & 7) << 4);
  unsigned l16 = lane * 16;
  asm volatile("" : "+v"(kvf_off), "+v"(w2f_off), "+v"(gs_off), "+v"(gc_off), "+v"(b1_off), "+v"(ks_off), "+v"(ko_rd), "+v"(l16));
  asm volatile("s_waitcnt vmcnt(6) lgkmcnt(0)\n\ts_barrier" : "+s"(tag), "+s"(dpair) :: "memory");
  tag += 1ull;
  u32x2 tagv = {(unsigned)tag, (unsigned)(tag >> 32)};
  OPAQUE(tagv);
#define TRYF(SENT, SO, Y, FORCE) do { if (!(SENT)) { const unsigned ib = __builtin_amdgcn_s_getreg(7 | (31 << 11)); \
      if ((FORCE) || ((ib & 15u) | ((ib >> 18) & 48u)) <= (Y)) { VMWAIT(Y); flag_put(sWS, SO, tagv); SENT = true; } } } while (0)
  bool wsent = false, ksent = false;
  {
    ldf(0);
    const unsigned wa = L_SW + (16 * (w >> 1) + 4 * hh + q4) * 128 + (w & 1) * 64 + blk * 32 + p4 * 8;
    const u32x2 wa0 = ldstr(wa), wa1 = ldstr(wa + 1024);
    STEP(0);
    st128_sc1(u32x4{wa0[0], wa0[1], wa1[0], wa1[1]}, rWS, l16, w2t_so);
  }
  PIECE(0); STEP(1); PIECE(1); STEP(2); PIECE(2);
  VMWAIT(4); BAR();
  {
    const unsigned ta = (8 * w + q4) * 128 + (lane >> 4) * 32 + p4 * 8;
    u32x2 k0 = ldstr(L_SK + 512 + ta), k1 = ldstr(L_SK + 1024 + ta), k2 = ldstr(L_SK + 1536 + ta);
    u32x2 v0 = ldstr(L_SV + ta), v1 = ldstr(L_SV + 512 + ta);
    u32x2 shk = lds64(L_TP + lane * 8);
    STEP(3);
    PIECE(3);
    BAR();
    PIECE(4);
    TRYF(wsent, kf_so + (WS_W2F - WS_KVF), 5, false);
    const float kr[11] = {bfhi(k0[0]), bflo(k0[1]), bfhi(k0[1]), bflo(k1[0]), bfhi(k1[0]), bflo(k1[1]), bfhi(k1[1]), bflo(k2[0]), bfhi(k2[0]), bflo(k2[1]), bfhi(k2[1])};
    const float tw0 = bflo(shk[0]), tw1 = bfhi(shk[0]), tw2 = bflo(shk[1]), tw3 = bfhi(shk[1]);
    float a[8]; unsigned pp[8];
#pragma unroll
    for (int j = 0; j < 8; ++j) pp[j] = pk(fmul1(kr[j], tw3), fmul1(kr[j + 1], tw2));
    PIECE(5);
    dot2x8(a, pp, ONES, ONES, ONES, ONES);
#pragma unroll
    for (int j = 0; j < 8; ++j) pp[j] = pk(a[j], fmul1(kr[j + 2], tw1));
    PIECE(6);
    dot2x8(a, pp, ONES, ONES, ONES, ONES);
#pragma unroll
    for (int j = 0; j < 8; ++j) pp[j] = pk(a[j], fmul1(kr[j + 3], tw0));
    PIECE(7);
    dot2x8(a, pp, ONES, ONES, ONES, ONES);
#pragma unroll
    for (int j = 0; j < 8; j += 2) { pp[j] = pk(a[j], 0.f); pp[j + 1] = pk(0.f, a[j + 1]); }
    dot2x8(a, pp, v0[0], v0[1], v1[0], v1[1]);
    sts128(L_KO + lane * 128 + ((w ^ (lane & 7)) << 4), u32x4{pk(a[7], a[6]), pk(a[5], a[4]), pk(a[3], a[2]), pk(a[1], a[0])});
    sts128(L_U + (fg >> 1) * U_SZ + ((fg & 1) * 2 + hh) * U_PL + r * 16, u32x4{0u, 0u, 0u, 0u});
    TRYF(wsent, kf_so + (WS_W2F - WS_KVF), 8, false);
    SB();
  }
  STEP(4); PIECE(8); STEP(5); PIECE(9); STEP(6); PIECE(10);
  VMWAIT(7); TRYF(wsent, kf_so + (WS_W2F - WS_KVF), 11, true); BAR();
  {
    const u32x4 o = lds128(ko_rd);
    STEP(7);
    st128_sc1(o, rWS, ks_off, WS_KVT + tt * 128);
  }
  PIECE(11); STEP(8); PIECE(12); STEP(9); PIECE(13); STEP(10); PIECE(14);
  VMWAIT(8); TRYF(ksent, kf_so, 4, false); BAR();
  u32x2 pf, pw, gs, gc; unsigned b1v = 0; u32x4 uld;
  STEP(11); PIECE(15); STEP(12);
  TRYF(ksent, kf_so, 5, false);
  asm volatile("buffer_load_dwordx2 %0, %1, %2, 0 offen sc1" : "=v"(pw) : "v"(w2f_off), "s"(sWS) : "memory");
  PIECE(16); STEP(13); PIECE(17); STEP(14); PIECE(18);
  VMWAIT(8); TRYF(ksent, kf_so, 9, true);
  BAR();
  u32x4 wf[28];
  const unsigned w2so = WS_W2T + (nt * 112 + 14 * w) * 2048;
  auto ldw2 = [&](int n0, int n1) {
#pragma unroll
    for (int n = n0; n < n1; ++n) { wf[n] = __builtin_amdgcn_raw_buffer_load_b128(rWS, l16 + (n & 3) * 1024, w2so + (n >> 2) * 4096, 16); SB(); }
  };
  STEP(15); PIECE(19);
  STEP(16); dma<L_SH, 0>(wb1, l16, sH, h_so);
  asm volatile("s_waitcnt vmcnt(5)" : "+v"(pw) :: "memory");
  if (!__all((((u64)pw[1] << 32) | pw[0]) == tag)) poll_flags(ws, w2f_off, tag);
  STEP(17); ldw2(0, 1);
  STEP(18); ldw2(1, 2);
  BAR();
  STEP(19);
  asm volatile("global_load_short_d16_hi %0, %1, %2" : "+v"(b1v) : "v"(b1_off), "s"(gb1) : "memory");
  asm volatile("buffer_load_dwordx2 %0, %1, %2, 0 offen sc1" : "=v"(gs) : "v"(gs_off), "s"(sWS) : "memory");
  asm volatile("buffer_load_dwordx2 %0, %1, %2, 0 offen sc1" : "=v"(gc) : "v"(gc_off), "s"(sWS) : "memory");
  STEP(20); ldw2(2, 3); STEP(21); ldw2(3, 4); STEP(22); ldw2(4, 5);
  VMWAIT(8);
  BAR();
  STEP(23); ldw2(5, 6);
  unsigned ed[4][3];
  {
    unsigned ra = L_SH + w * 1024 + lane * 4;
    OPAQUE(ra);
#pragma unroll
    for (int j = 0; j < 4; ++j)
#pragma unroll
      for (int e = 0; e < 3; ++e) ed[j][e] = *(const LAS unsigned*)(ra + j * 256 + e * 4);
  }
  STEP(24);
  asm volatile("buffer_load_dwordx2 %0, %1, %2, 0 offen sc1" : "=v"(pf) : "v"(kvf_off), "s"(sWS) : "memory");
  STEP(25); ldw2(6, 7);
  STEP(26); ldw2(7, 8);
  STEP(27);
  asm volatile("s_waitcnt vmcnt(7)" : "+v"(b1v), "+v"(gs), "+v"(gc) :: "memory");
  if (!__all(gs[1] == (unsigned)tag && gc[1] == (unsigned)tag)) { gs[0] = poll_gran(ws, gs_off, (unsigned)tag); gc[0] = poll_gran(ws, gc_off, (unsigned)tag); }
  const unsigned u_so = WS_KVT + (ch0 + tg) * 4096 + fg * 1024;
  {
    if (fg == 0) *(LAS unsigned*)(L_ST + (64 * tg + lane) * 4) = gs[0];
    const unsigned nm = gs[0] ^ 0x8000u;
    const unsigned bm = __builtin_amdgcn_perm(nm, nm, 0x01000100u);
    const float c = __uint_as_float(gc[0]), chi = rbf(c), c1 = c - chi, cmid = rbf(c1);
    const u32x4 A = {pk(chi, cmid), pk(c1 - cmid, __uint_as_float(b1v)), 0u, 0u};
    acc0 = mfma(A, u32x4{hh ? 0u : bm, hh ? 0u : nm, 0u, 0u}, acc0);
    acc1 = mfma(A, u32x4{hh ? bm : 0u, hh ? nm : 0u, 0u, 0u}, acc1);
    asm volatile("s_waitcnt vmcnt(2)" : "+v"(pf) :: "memory");
    asm volatile("buffer_load_dwordx4 %0, %1, %2, %3 offen sc1" : "=v"(uld) : "v"(l16), "s"(sWS), "s"(u_so) : "memory");
    ldw2(8, 10);
    const unsigned so = WS_HT + ((4 * mt128 + 2 * tg) * 112 + 8 * ft + 2 * fg) * 1024;
    unsigned wa = L_E + 256 + tg * E_SZ + fg * 4096 + lane * 16;
    OPAQUE(wa);
#pragma unroll
    for (int q = 0; q < 4; ++q) {
      sts128(wa + q * 1024, u32x4{ed[q][0], ed[q][1], __builtin_amdgcn_alignbit(ed[q][1], ed[q][0], 16), __builtin_amdgcn_alignbit(ed[q][2], ed[q][1], 16)});
      u32x4 hv;
#pragma unroll
      for (int e = 0; e < 4; ++e) {
        float a0 = (q < 2 ? acc0 : acc1)[8 * (q & 1) + 2 * e], a1 = (q < 2 ? acc0 : acc1)[8 * (q & 1) + 2 * e + 1];
        a0 *= __builtin_fabsf(a0); OPAQUE(a0);
        a1 *= __builtin_fabsf(a1);
        const s16x2 m = __builtin_elementwise_max(__builtin_bit_cast(s16x2, pk(a0, a1)), s16x2{0, 0});
        hv[e] = __builtin_bit_cast(unsigned, m);
      }
      st128_sc1(hv, rWS, l16 + (q & 1) * 1024, so + (q >> 1) * 112 * 1024);
    }
  }
  asm volatile("s_waitcnt vmcnt(6)" : "+v"(uld) :: "memory");
  if (!__all((((u64)pf[1] << 32) | pf[0]) == tag)) {
    poll_flags(ws, kvf_off, tag);
    asm volatile("buffer_load_dwordx4 %0, %1, %2, %3 offen sc1\n\ts_waitcnt vmcnt(0)" : "=&v"(uld) : "v"(l16), "s"(sWS), "s"(u_so) : "memory");
  }
  {
    const int n = 64 * fg + lane;
    sts128(L_U + tg * U_SZ + (n & 3) * U_PL + ((n >> 2) + 32) * 16, uld);
    if (fg == 0) {
      const unsigned h0 = __builtin_amdgcn_readfirstlane(ed[0][0]), h1 = __builtin_amdgcn_readfirstlane(__builtin_amdgcn_alignbit(ed[0][1], ed[0][0], 16));
      const int l = lane & 15;
      sts128(L_E + tg * E_SZ + l * 16, u32x4{0u, l == 15 ? h0 : 0u, l == 15 ? h0 << 16 : 0u, l == 15 ? h1 : l == 14 ? h0 << 16 : 0u});
    }
  }
  ldw2(10, 12);
  BAR();
  const int tok = threadIdx.x >> 4, quad = threadIdx.x & 15;
  const unsigned hf_off = WS_HF + (mt64 * 56 + 7 * w + (lane < 7 ? lane : 6)) * FSTR;
  const unsigned xo = ((64 * mt64 + tok) * D_ + 64 * nt + 4 * quad) * 2;
  u32x2 hf, xr0, xr1, b2r;
  constexpr int WIN = HWIN, NH0 = (16 - HCHK) * HPS;
  u32x4 hb[WIN];
  const unsigned hso = WS_HT + (2 * mt64 * 112 + 14 * w) * 1024;
  auto ldh = [&](int n) {
    const int mt = n / 14, ks = n % 14;
    hb[n % WIN] = __builtin_amdgcn_raw_buffer_load_b128(rWS, l16, hso + mt * 112 * 1024 + ks * 1024, 16);
    SB();
  };
  {
    f32x16 c0, c1;
#pragma unroll
    for (int i = 0; i < 16; ++i) { c0[i] = 0.f; c1[i] = 0.f; }
    unsigned ea0 = L_E + tg * E_SZ + 8 * (32 + r - 8 * hh - 7 + 256 * fg - 16), ea1 = ea0 + 32, ea2 = ea0 + 8192, ea3 = ea0 + 8224;
    unsigned ua = L_U + tg * U_SZ + hh * U_PL + 16 * (r + 32 - 8 * fg - 7);
    OPAQUE(ea0); OPAQUE(ea1); OPAQUE(ea2); OPAQUE(ea3); OPAQUE(ua);
    u32x4 A0[2], A1[2], B0[2], B1[2];
    auto ldca = [&](int st) {
      const int dd = st >> 1, s = st & 1;
      const unsigned eo = 8 * (32 * dd + 16 * (1 - s)), uo = 2 * s * U_PL + 16 * (7 - dd);
      u32x2 x0 = lds64(ea0 + eo), x1 = lds64(ea1 + eo);
      B0[st & 1] = lds128(ua + uo);
      A0[st & 1] = u32x4{x0[0], x0[1], x1[0], x1[1]};
    };
    auto ldcb = [&](int st) {
      const int dd = st >> 1, s = st & 1;
      const unsigned eo = 8 * (32 * dd + 16 * (1 - s)), uo = 2 * s * U_PL + 16 * (7 - dd);
      B1[st & 1] = lds128(ua + uo + 512);
      u32x2 y0 = lds64(ea2 + eo), y1 = lds64(ea3 + eo);
      A1[st & 1] = u32x4{y0[0], y0[1], y1[0], y1[1]};
    };
    ldca(0); ldcb(0);
    SB();
    bool hsent = false;
#pragma unroll
    for (int st = 0; st < 16; ++st) {
      c0 = mfma(A0[st & 1], B0[st & 1], c0); asm volatile("" : "+v"(c0)); SB();
      if (st + 1 < 16) ldca(st + 1);
      SB();
      c1 = mfma(A0[st & 1], B1[st & 1], c1); asm volatile("" : "+v"(c1)); SB();
      if (st + 1 < 16) ldcb(st + 1);
      SB();
      c1 = mfma(A1[st & 1], B0[st & 1], c1); asm volatile("" : "+v"(c1)); SB();
      if (st >= HF0 && st <= SIGF && (st & 1) && !hsent) {
        const int younger = 2 + (st < 8 ? 2 * st : 16);
        const unsigned ib = __builtin_amdgcn_s_getreg(7 | (31 << 11));
        if (st == SIGF || ((ib & 15u) | ((ib >> 18) & 48u)) <= (unsigned)younger) {
          VMWAIT(younger);
          flag_put(sWS, hf_so, tagv);
          hsent = true;
        }
      }
      if (st < 8) ldw2(12 + 2 * st, 14 + 2 * st);
      if (st == 8) {
        asm volatile("buffer_load_dwordx2 %0, %1, %2, 0 offen" : "=v"(xr0) : "v"(xo), "s"(sX) : "memory");
        asm volatile("buffer_load_dwordx2 %0, %1, %2, %3 offen" : "=v"(xr1) : "v"(xo), "s"(sX), "s"(32 * 896) : "memory");
        asm volatile("global_load_dwordx2 %0, %1, %2" : "=v"(b2r) : "v"((64 * nt + 4 * quad) * 2), "s"(gb2) : "memory");
      }
      if (st == HPOLL) asm volatile("buffer_load_dwordx2 %0, %1, %2, 0 offen sc1" : "=v"(hf) : "v"(hf_off), "s"(sWS) : "memory");
      if (st == HCHK) {
        asm volatile("s_waitcnt vmcnt(0)" : "+v"(hf), "+v"(xr0), "+v"(xr1), "+v"(b2r) :: "memory");
        if (!__all((((u64)hf[1] << 32) | hf[0]) == tag)) poll_flags(ws, hf_off, tag);
      }
      if (st >= HCHK) {
#pragma unroll
        for (int n = (st - HCHK) * HPS; n < (st - HCHK + 1) * HPS; ++n) ldh(n);
      }
    }
    const unsigned pb = L_CP + w * P_SZ + lane * 16;
#pragma unroll
    for (int g4 = 0; g4 < 4; ++g4) {
      *(LAS f32x4*)(pb + g4 * 1088) = f32x4{c0[4 * g4], c0[4 * g4 + 1], c0[4 * g4 + 2], c0[4 * g4 + 3]};
      *(LAS f32x4*)(pb + 4352 + g4 * 1088) = f32x4{c1[4 * g4], c1[4 * g4 + 1], c1[4 * g4 + 2], c1[4 * g4 + 3]};
    }
  }
  BAR();
  {
    const int n = 32 * w + r, a = n >> 2, g4 = n & 3;
    const unsigned po = L_CP + hh * 4 * P_SZ + (a >> 5) * 4352 + g4 * 1088 + (a & 31) * 16;
    f32x4 lo = *(const LAS f32x4*)po, hi = *(const LAS f32x4*)(po + 512);
#pragma unroll
    for (int q = 1; q < 4; ++q) {
      lo += *(const LAS f32x4*)(po + q * P_SZ);
      hi += *(const LAS f32x4*)(po + q * P_SZ + 512);
    }
    const u32x4 kvc = lds128(L_U + hh * U_SZ + g4 * U_PL + (a + 32) * 16);
    const unsigned dlo = hh ? dpair >> 16 : dpair & 0xffffu, dhi = dlo << 16;
    float pd[8], yv[8];
    { const unsigned kk[8] = {kvc[3], kvc[2], kvc[1], kvc[0], kvc[3], kvc[2], kvc[1], kvc[0]}; dot2x8(pd, kk, dhi, dhi, dlo, dlo); }
    { const unsigned pp[8] = {pk(lo[0], pd[0]), pk(lo[1], pd[4]), pk(lo[2], pd[1]), pk(lo[3], pd[5]), pk(hi[0], pd[2]), pk(hi[1], pd[6]), pk(hi[2], pd[3]), pk(hi[3], pd[7])};
      dot2x8(yv, pp, ONES, ONES, ONES, ONES); }
    const u32x4 yo = {pk(yv[0], yv[1]), pk(yv[2], yv[3]), pk(yv[4], yv[5]), pk(yv[6], yv[7])};
    st128_sc1(yo, rWS, (ch0 + hh) * 4096 + n * 16, WS_YT);
  }
#pragma unroll
  for (int n = NH0; n < WIN; ++n) ldh(n);
  auto gate = [&](unsigned ra, float r2, unsigned (&gt)[2]) {
    f32x4 sm = *(const LAS f32x4*)ra;
#pragma unroll
    for (int q = 1; q < 8; ++q) sm += *(const LAS f32x4*)(ra + q * G_SZ);
    const unsigned oa = pk(fmaf(sm[0], r2, bflo(b2r[0])), fmaf(sm[1], r2, bfhi(b2r[0]))), ob = pk(fmaf(sm[2], r2, bflo(b2r[1])), fmaf(sm[3], r2, bfhi(b2r[1])));
    const float o[4] = {bflo(oa), bfhi(oa), bflo(ob), bfhi(ob)};
    float sv[4];
#pragma unroll
    for (int e = 0; e < 4; ++e) sv[e] = o[e] * __builtin_amdgcn_rcpf(1.f + __builtin_amdgcn_exp2f(o[e] * -1.4426950408889634f));
    gt[0] = pk(sv[0], sv[1]); gt[1] = pk(sv[2], sv[3]);
  };
  auto xnorm = [&](u32x2 xr, unsigned st, unsigned (&xn)[2], float& r2, float* xf) {
    const float m = bflo(st), rd = __builtin_amdgcn_rcpf(bfhi(st));
    xf[0] = bflo(xr[0]); xf[1] = bfhi(xr[0]); xf[2] = bflo(xr[1]); xf[3] = bfhi(xr[1]);
    const unsigned da = pk(xf[0] - m, xf[1] - m), db = pk(xf[2] - m, xf[3] - m);
    xn[0] = pk(bflo(da) * rd, bfhi(da) * rd); xn[1] = pk(bflo(db) * rd, bfhi(db) * rd);
    r2 = rd * rd;
  };
  unsigned g0[2], g1[2], xn0[2], xn1[2];
  float r20, r21, xf[8];
  unsigned rp0 = L_G0 + quad * G_H + tok * 16, rp1 = rp0 - (L_G0 - L_G1), yra = L_Y + 4 * quad * 132 + tok * 2;
  const unsigned yf_off = WS_YF + ((nt * 8 + (mt64 >> 2)) * 32 + 4 * w + (lane & 3)) * FSTR;
  u32x2 yf; u32x4 yb;
  const unsigned yvo = (lane >> 3) * 4096 + (lane & 7) * 16, yso = WS_YT + (64 * nt + 8 * w) * 4096 + mt64 * 128;
  {
    f32x16 ca[2], cb[2];
    f32x4 gp[4], gq[4]; float gs4[4], ge[4];
    const float nl2e = -1.4426950408889634f;
#pragma unroll
    for (int i = 0; i < 16; ++i) { ca[0][i] = 0.f; cb[0][i] = 0.f; ca[1][i] = 0.f; cb[1][i] = 0.f; }
    unsigned pb0 = L_G0 + w * G_SZ + hh * G_H + r * 16, pb1 = pb0 - (L_G0 - L_G1);
    OPAQUE(pb0); OPAQUE(pb1);
    auto wrp = [&](unsigned pb, const f32x16& c, int g4, int t) { *(LAS f32x4*)(pb + t * G_T + g4 * G_G) = f32x4{c[4 * g4], c[4 * g4 + 1], c[4 * g4 + 2], c[4 * g4 + 3]}; };
#pragma unroll
    for (int n = 0; n < 28; ++n) {
      const int mt = n / 14, ks = n % 14;
      if (n == NH0) { VMWAIT(WIN - 1); flag_put(sWS, yf_so, tagv); }
      ca[mt] = mfma(wf[2 * ks], hb[n % WIN], ca[mt]); asm volatile("" : "+v"(ca[mt])); SB();
      if (n >= 1 && n - 1 + WIN < 28) ldh(n - 1 + WIN);
      if (n >= 14 && n < 18) { wrp(pb0, ca[0], n - 14, 0); SB(); }
      cb[mt] = mfma(wf[2 * ks + 1], hb[n % WIN], cb[mt]); asm volatile("" : "+v"(cb[mt])); SB();
      if (n == 1) {
        const unsigned st0 = *(const LAS unsigned*)(L_ST + (64 * hsel + tok) * 4), st1 = *(const LAS unsigned*)(L_ST + (64 * hsel + 32 + tok) * 4);
        xnorm(xr0, st0, xn0, r20, xf); xnorm(xr1, st1, xn1, r21, xf + 4);
        asm volatile("" : "+v"(xn0[0]), "+v"(xn0[1]), "+v"(xn1[0]), "+v"(xn1[1]), "+v"(r20), "+v"(r21), "+v"(rp0), "+v"(rp1), "+v"(yra)); PIN8(xf); SB();
      }
      if (n >= 14 && n < 18) { wrp(pb0, cb[0], n - 14, 1); SB(); }
      if (n == 20) { BAR(); }
      if (n == 20 || n == 21) {
#pragma unroll
        for (int q = 0; q < 4; ++q) gp[q] = *(const LAS f32x4*)(rp0 + (4 * (n - 20) + q) * G_SZ);
        SB();
      }
      if (n == 21 || n == 22) {
#pragma unroll
        for (int e = 0; e < 4; ++e) { const float t = fadd1(fadd1(gq[0][e], gq[1][e]), fadd1(gq[2][e], gq[3][e])); gs4[e] = n == 21 ? t : fadd1(gs4[e], t); }
        asm volatile("" : "+v"(gs4[0]), "+v"(gs4[1]), "+v"(gs4[2]), "+v"(gs4[3])); SB();
      }
      if (n == 20 || n == 21) {
#pragma unroll
        for (int q = 0; q < 4; ++q) gq[q] = gp[q];
      }
      if (n == 23) {
        const unsigned oa = pk(ffma1(gs4[0], r20, bflo(b2r[0])), ffma1(gs4[1], r20, bfhi(b2r[0]))), ob = pk(ffma1(gs4[2], r20, bflo(b2r[1])), ffma1(gs4[3], r20, bfhi(b2r[1])));
        gs4[0] = bflo(oa); gs4[1] = bfhi(oa); gs4[2] = bflo(ob); gs4[3] = bfhi(ob);
        asm volatile("" : "+v"(gs4[0]), "+v"(gs4[1]), "+v"(gs4[2]), "+v"(gs4[3])); SB();
      }
      if (n == 24) {
#pragma unroll
        for (int e = 0; e < 4; ++e) ge[e] = __builtin_amdgcn_exp2f(fmul1(gs4[e], nl2e));
        asm volatile("" : "+v"(ge[0]), "+v"(ge[1]), "+v"(ge[2]), "+v"(ge[3])); SB();
      }
      if (n == 25) {
#pragma unroll
        for (int e = 0; e < 4; ++e) ge[e] = __builtin_amdgcn_rcpf(1.f + ge[e]);
        asm volatile("" : "+v"(ge[0]), "+v"(ge[1]), "+v"(ge[2]), "+v"(ge[3])); SB();
      }
      if (n == 26) {
        g0[0] = pk(fmul1(gs4[0], ge[0]), fmul1(gs4[1], ge[1])); g0[1] = pk(fmul1(gs4[2], ge[2]), fmul1(gs4[3], ge[3]));
        asm volatile("" : "+v"(g0[0]), "+v"(g0[1])); SB();
      }
      if (n == YPOLL) asm volatile("buffer_load_dwordx2 %0, %1, %2, 0 offen sc1" : "=v"(yf) : "v"(yf_off), "s"(sWS) : "memory");
      if (n == YPOLL + YLAG) {
        asm volatile("s_waitcnt vmcnt(0)" : "+v"(yf) :: "memory");
        asm volatile("buffer_load_dwordx4 %0, %1, %2, %3 offen sc1" : "=v"(yb) : "v"(yvo), "s"(sWS), "s"(yso) : "memory");
      }
    }
#pragma unroll
    for (int g4 = 0; g4 < 4; ++g4) { wrp(pb1, ca[1], g4, 0); wrp(pb1, cb[1], g4, 1); }
  }
  asm volatile("s_waitcnt vmcnt(0)" : "+v"(yb) :: "memory");
  if (!__all((((u64)yf[1] << 32) | yf[0]) == tag)) {
    poll_flags(ws, yf_off, tag);
    asm volatile("buffer_load_dwordx4 %0, %1, %2, %3 offen sc1\n\ts_waitcnt vmcnt(0)" : "=&v"(yb) : "v"(yvo), "s"(sWS), "s"(yso) : "memory");
  }
  {
    const unsigned ya = L_Y + (8 * w + (lane >> 3)) * 132 + (lane & 7) * 16;
#pragma unroll
    for (int e = 0; e < 4; ++e) *(LAS unsigned*)(ya + 4 * e) = yb[e];
  }
  BAR();
  {
    unsigned yv[8];
#pragma unroll
    for (int e = 0; e < 8; ++e) yv[e] = *(const LAS u16*)(yra + (e & 3) * 132 + (e >> 2) * 64);
    gate(rp1, r21, g1);
    float t[8]; unsigned pp[8];
#pragma unroll
    for (int e = 1; e < 8; e += 2) yv[e] <<= 16;
    dot2x8(t, yv, xn0[0], xn0[1], xn1[0], xn1[1]);
#pragma unroll
    for (int e = 0; e < 8; e += 2) { pp[e] = pk(t[e], 0.f); pp[e + 1] = pk(0.f, t[e + 1]); }
    dot2x8(t, pp, g0[0], g0[1], g1[0], g1[1]);
#pragma unroll
    for (int e = 0; e < 8; ++e) pp[e] = pk(t[e], xf[e]);
    dot2x8(t, pp, ONES, ONES, ONES, ONES);
    const auto rO = __builtin_amdgcn_make_buffer_rsrc(gout, 0, S_ * D_ * 2, 0x00020000);
    __builtin_amdgcn_raw_buffer_store_b64(u32x2{pk(t[0], t[1]), pk(t[2], t[3])}, rO, xo, 0, 16);
    __builtin_amdgcn_raw_buffer_store_b64(u32x2{pk(t[4], t[5]), pk(t[6], t[7])}, rO, xo, 32 * 896, 16);
  }
}

extern "C" void kernel_launch(void* const* d_in, const int* in_sizes, int n_in, void* d_out, int out_size, void* d_ws, size_t ws_size,
                              hipStream_t stream) {
  (void)in_sizes; (void)n_in; (void)out_size; (void)ws_size;
  (void)hipFuncSetAttribute((const void*)fused_h3_mlp, hipFuncAttributeMaxDynamicSharedMemorySize, LDS_BYTES);
  fused_h3_mlp<<<dim3(8, 8, 4), 512, LDS_BYTES, stream>>>((const u16*)d_in[0], (const u16*)d_in[1], (const u16*)d_in[2], (const u16*)d_in[3],
                                                         (const u16*)d_in[4], (const u16*)d_in[5], (const u16*)d_in[7], (const u16*)d_in[8],
                                                         (const u16*)d_in[9], (const u16*)d_in[10], (u16*)d_out, (char*)d_ws);
}
```

```cpp
#include <hip/hip_runtime.h>
#include <hip/hip_bf16.h>
#include <stdint.h>
#include <type_traits>

typedef unsigned short u16;
typedef unsigned long long u64;
typedef __bf16 bf16x8 __attribute__((ext_vector_type(8)));
typedef __bf16 bf16x2 __attribute__((ext_vector_type(2)));
typedef short s16x2 __attribute__((ext_vector_type(2)));
typedef short s16x4 __attribute__((ext_vector_type(4)));
typedef float f32x2 __attribute__((ext_vector_type(2)));
typedef float f32x4 __attribute__((ext_vector_type(4)));
typedef float f32x16 __attribute__((ext_vector_type(16)));
typedef unsigned u32x2 __attribute__((ext_vector_type(2)));
typedef unsigned u32x4 __attribute__((ext_vector_type(4)));
typedef int i32x4 __attribute__((ext_vector_type(4)));
#define GAS __attribute__((address_space(1)))
#define LAS __attribute__((address_space(3)))
#define DI __device__ __forceinline__
#pragma clang diagnostic ignored "-Wint-to-pointer-cast"

constexpr int S_ = 2048, D_ = 448, F_ = 1792;
constexpr int NWG = 224;
constexpr unsigned FSTR = 256;
constexpr unsigned WS_EPOCH = 0;
constexpr unsigned WS_KVF = 4096;
constexpr unsigned WS_HF = WS_KVF + 1792 * FSTR;
constexpr unsigned WS_YF = WS_HF + 1792 * FSTR;
constexpr unsigned WS_W2F = WS_YF + 1792 * FSTR;
constexpr unsigned WS_STG = WS_W2F + 1792 * FSTR;
constexpr unsigned WS_CSG = WS_STG + 2048 * 8;
constexpr unsigned WS_KVT = 2u << 20;
constexpr unsigned WS_YT = 4u << 20;
constexpr unsigned WS_HT = 6u << 20;
constexpr unsigned WS_W2T = WS_HT + 64u * 112u * 1024u;
constexpr unsigned WS_END = WS_W2T + 7u * 112u * 2048u;
static_assert(WS_CSG + 1792 * 8 <= WS_KVT, "ws");
constexpr unsigned WS_DBG = 16u << 20;
constexpr unsigned L_X = 49152;
constexpr unsigned L_W = 0;
constexpr unsigned L_E = 98304;
constexpr unsigned E_SZ = 16640;
constexpr unsigned U_PL = 1568, U_SZ = 4 * U_PL;
constexpr unsigned L_SH = L_E + 2 * E_SZ;
constexpr unsigned L_SS = L_SH + 8192 + 64;
constexpr unsigned L_KV = L_SS + 1024;
constexpr unsigned L_Y = L_KV;
constexpr unsigned L_ST = L_KV + 9216;
constexpr unsigned L_U = L_ST + 1024;
constexpr unsigned P_SZ = 8704;
constexpr unsigned L_CP = 0;
constexpr unsigned L_G0 = 69632;
constexpr unsigned G_SZ = 8448;
constexpr unsigned L_G1 = 0;
constexpr unsigned L_SK = L_U;
constexpr unsigned L_TP = L_U + 9216;
constexpr unsigned L_SV = L_KV;
constexpr unsigned L_KO = L_KV;
constexpr unsigned L_SW = L_SH;
constexpr unsigned G_H = 528, G_G = 1056, G_T = 4224;
constexpr unsigned LDS_BYTES = L_U + 2 * U_SZ;
static_assert(L_G0 + 8 * G_SZ <= L_KV && L_U % 16 == 0, "lds");
static_assert(LDS_BYTES <= 163840, "lds");

constexpr unsigned SPIN_CAP = 1u << 18;
constexpr unsigned ONES = 0x3f803f80u, ONE_HI = 0x3f800000u;

DI float bflo(unsigned v) { return __uint_as_float(v << 16); }
DI float bfhi(unsigned v) { return __uint_as_float(v & 0xffff0000u); }
DI unsigned pk(float lo, float hi) { f32x2 f = {lo, hi}; return __builtin_bit_cast(unsigned, __builtin_convertvector(f, bf16x2)); }
DI float fmul1(float a, float b) { float d; asm("v_mul_f32 %0, %1, %2" : "=v"(d) : "v"(a), "v"(b)); return d; }
DI float fadd1(float a, float b) { float d; asm("v_add_f32 %0, %1, %2" : "=v"(d) : "v"(a), "v"(b)); return d; }
DI float ffma1(float a, float b, float c) { float d; asm("v_fma_f32 %0, %1, %2, %3" : "=v"(d) : "v"(a), "v"(b), "v"(c)); return d; }
DI float rbf(float a) { return bfhi(pk(a, a)); }
DI float dot2(unsigned a, unsigned b, float c) { return __builtin_amdgcn_fdot2_f32_bf16(__builtin_bit_cast(bf16x2, a), __builtin_bit_cast(bf16x2, b), c, false); }
DI void dot2x8(float (&d)[8], const unsigned (&a)[8], unsigned b0, unsigned b1, unsigned b2, unsigned b3) {
  asm volatile("v_dot2_f32_bf16 %0, %8, %16, 0\n\tv_dot2_f32_bf16 %1, %9, %16, 0\n\tv_dot2_f32_bf16 %2, %10, %17, 0\n\tv_dot2_f32_bf16 %3, %11, %17, 0\n\t"
               "v_dot2_f32_bf16 %4, %12, %18, 0\n\tv_dot2_f32_bf16 %5, %13, %18, 0\n\tv_dot2_f32_bf16 %6, %14, %19, 0\n\tv_dot2_f32_bf16 %7, %15, %19, 0\n\ts_nop 2"
               : "=&v"(d[0]), "=&v"(d[1]), "=&v"(d[2]), "=&v"(d[3]), "=&v"(d[4]), "=&v"(d[5]), "=&v"(d[6]), "=&v"(d[7])
               : "v"(a[0]), "v"(a[1]), "v"(a[2]), "v"(a[3]), "v"(a[4]), "v"(a[5]), "v"(a[6]), "v"(a[7]), "v"(b0), "v"(b1), "v"(b2), "v"(b3));
}
DI f32x16 mfma(u32x4 a, u32x4 b, f32x16 c) { return __builtin_amdgcn_mfma_f32_32x32x16_bf16(__builtin_bit_cast(bf16x8, a), __builtin_bit_cast(bf16x8, b), c, 0, 0, 0); }
DI u32x4 lds128(unsigned a) { return *(const LAS u32x4*)a; }
DI u32x2 lds64(unsigned a) { return *(const LAS u32x2*)a; }
DI u32x2 ldstr(unsigned a) { return __builtin_bit_cast(u32x2, __builtin_amdgcn_ds_read_tr16_b64_v4i16((LAS s16x4*)a)); }
DI void sts128(unsigned a, u32x4 v) { *(LAS u32x4*)a = v; }
DI i32x4 mksrd(const void* p, unsigned bytes) {
  u64 a = (u64)p;
  i32x4 r = {(int)(unsigned)a, (int)((unsigned)(a >> 32) & 0xffffu), (int)bytes, 0x00020000};
  return r;
}
#define VMWAIT(n) asm volatile("s_waitcnt vmcnt(%0)" ::"i"(n) : "memory")
#define BAR() asm volatile("s_waitcnt lgkmcnt(0)\n\ts_barrier" ::: "memory")
#define OPAQUE(x) asm volatile("" : "+v"(x))
#define SB() __builtin_amdgcn_sched_barrier(0)
#define ROR(x, n) __uint_as_float(__builtin_amdgcn_update_dpp(0u, __float_as_uint(x), 0x120 + (n), 0xf, 0xf, false))
#define PIN8(a) asm volatile("" : "+v"(a[0]), "+v"(a[1]), "+v"(a[2]), "+v"(a[3]), "+v"(a[4]), "+v"(a[5]), "+v"(a[6]), "+v"(a[7]))
template <int LOFF, int GOFF> DI void dma(unsigned wb, unsigned voff, i32x4 srd, unsigned soff) {
  asm volatile("s_add_i32 m0, %0, %4\n\ts_nop 0\n\tbuffer_load_dwordx4 %1, %2, %3 offen offset:%5 lds"
               ::"s"(wb), "v"(voff), "s"(srd), "s"(soff), "i"(LOFF - GOFF), "i"(GOFF) : "scc");
}
template <typename R> DI void st128_sc1(u32x4 v, R rsrc, unsigned voff, unsigned soff) {
  __builtin_amdgcn_raw_buffer_store_b128(v, rsrc, voff, soff, 16);
  asm volatile("s_nop 1" ::"v"(v) : "memory");
}
DI void flag_store(char* ws, unsigned off, u64 tag, int lane) {
  if (lane == 0) __hip_atomic_store((GAS u64*)(ws + off), tag, __ATOMIC_RELAXED, __HIP_MEMORY_SCOPE_AGENT);
}
DI void flag_put(i32x4 srd, unsigned soff, u32x2 tagv) { asm volatile("buffer_store_dwordx2 %0, off, %1, %2 sc1" ::"v"(tagv), "s"(srd), "s"(soff) : "memory"); }
DI void poll_flags(char* ws, unsigned off, u64 tag) {
#pragma unroll 1
  for (unsigned spins = 0; spins < SPIN_CAP; ++spins) {
    u64 f = __hip_atomic_load((GAS u64*)(ws + off), __ATOMIC_RELAXED, __HIP_MEMORY_SCOPE_AGENT);
    if (__all(f == tag)) break;
    __builtin_amdgcn_s_sleep(1);
  }
  asm volatile("" ::: "memory");
}

DI unsigned poll_gran(char* ws, unsigned off, unsigned tag) {
  u64 f = 0;
#pragma unroll 1
  for (unsigned spins = 0; spins < SPIN_CAP; ++spins) {
    f = __hip_atomic_load((GAS u64*)(ws + off), __ATOMIC_RELAXED, __HIP_MEMORY_SCOPE_AGENT);
    if (__all((unsigned)(f >> 32) == tag)) break;
    __builtin_amdgcn_s_sleep(1);
  }
  asm volatile("" ::: "memory");
  return (unsigned)f;
}

#define STAMP(i) do {} while (0)
#define PART42 1
#define SIGF 7
#define HF0 3
#define NOW2 0
#define KSW 5
#define HPOLL 10
#define HCHK 13
#define HPS 3
#define TAUX 16
#define HAUX 16
#define ROT1 0
#define ROT2 0
#define ABL 0
#define YPOLL 15
#define YLAG 6
#define OUTAUX 16
#define HFIRST 6
#define HWIN 12

__global__ __launch_bounds__(512, 2) void fused_h3_mlp(const u16* __restrict__ gx, const u16* __restrict__ gk, const u16* __restrict__ gv,
                                                      const u16* __restrict__ gshk, const u16* __restrict__ gssk, const u16* __restrict__ gsD,
                                                      const u16* __restrict__ gw1, const u16* __restrict__ gb1, const u16* __restrict__ gw2,
                                                      const u16* __restrict__ gb2, u16* __restrict__ gout, char* __restrict__ ws) {
  STAMP(0);
  const int g = blockIdx.x, yy = blockIdx.y, zz = blockIdx.z;
  const int w = __builtin_amdgcn_readfirstlane(threadIdx.x >> 6);
  const int lane = threadIdx.x & 63, r = lane & 31, hh = lane >> 5;
  const i32x4 sWS = mksrd(ws, WS_END);
  asm volatile("" ::"s"(gx), "s"(gk), "s"(gv), "s"(gshk), "s"(gssk), "s"(gsD), "s"(gw1), "s"(gb1), "s"(gw2), "s"(gb2), "s"(gout), "s"(ws));
  u64 tag;
  if (yy == 7) {
    const int a = g + 8 * zz;
    const i32x4 sX = mksrd(gx, S_ * D_ * 2), sW1 = mksrd(gw1, D_ * F_ * 2);
    u32x4 xr[8], wv[7];
    const int row0 = 256 * g + 64 * zz + 8 * w;
    const unsigned xo = lane < 56 && !(ABL & 4) ? lane * 16 : 0x7fffffffu;
#pragma unroll
    for (int j = 0; j < 8; ++j)
      asm volatile("buffer_load_dwordx4 %0, %1, %2, %3 offen" : "=v"(xr[j]) : "v"(xo), "s"(sX), "s"((row0 + j) * 896) : "memory");
    const int ci = (g >> 1) + 4 * zz, cb = 14 * (g & 1) + ci;
    const unsigned wo = ci < 14 && !(ABL & 4) ? (lane >> 3) * 3584 + (lane & 7) * 16 + cb * 128 : 0x7fffffffu;
#pragma unroll
    for (int j = 0; j < 7; ++j)
      asm volatile("buffer_load_dwordx4 %0, %1, %2, %3 offen" : "=v"(wv[j]) : "v"(wo), "s"(sW1), "s"((56 * w + 8 * j) * 3584) : "memory");
    asm volatile("s_load_dwordx2 %0, %1, 0x0\n\ts_waitcnt lgkmcnt(0)" : "=s"(tag) : "s"(ws) : "memory");
    tag += 1ull;
    {
      float v[16];
#pragma unroll
      for (int j = 0; j < 8; ++j) {
        asm volatile("s_waitcnt vmcnt(%1)" : "+v"(xr[j]) : "i"(14 - j) : "memory");
        float s = 0.f, q = 0.f;
#pragma unroll
        for (int e = 0; e < 4; ++e) { s = dot2(xr[j][e], ONES, s); q = dot2(xr[j][e], xr[j][e], q); }
        v[j] = s; v[8 + j] = q;
        asm volatile("" : "+v"(v[j]), "+v"(v[8 + j]));
      }
#pragma unroll
      for (int j = 0; j < 8; ++j) { auto t = __builtin_amdgcn_permlane32_swap(__float_as_uint(v[j]), __float_as_uint(v[8 + j]), false, false); v[j] = __uint_as_float(t[0]) + __uint_as_float(t[1]); }
#pragma unroll
      for (int j = 0; j < 4; ++j) { auto t = __builtin_amdgcn_permlane16_swap(__float_as_uint(v[j]), __float_as_uint(v[4 + j]), false, false); v[j] = __uint_as_float(t[0]) + __uint_as_float(t[1]); }
#pragma unroll
      for (int j = 0; j < 4; ++j) { v[j] += ROR(v[j], 8); v[j] += ROR(v[j], 4); v[j] += ROR(v[j], 2); v[j] += ROR(v[j], 1); }
      const float mine = (lane & 2) ? ((lane & 1) ? v[3] : v[2]) : ((lane & 1) ? v[1] : v[0]);
      const auto t = __builtin_amdgcn_permlane32_swap(__float_as_uint(mine), __float_as_uint(mine), false, false);
      const float sq = __uint_as_float(t[1]);
      const float mean = mine * (1.f / D_);
      const float var = fmaxf(sq * (1.f / D_) - mean * mean, 0.f);
      const float den = __builtin_amdgcn_sqrtf(rbf(rbf(var) + 1e-5f));
      if ((lane & 44) == 0)
        __hip_atomic_store((GAS u64*)(ws + WS_STG + (row0 + ((lane >> 4) & 1) * 4 + (lane & 3)) * 8), ((u64)(unsigned)tag << 32) | pk(mean, den), __ATOMIC_RELAXED, __HIP_MEMORY_SCOPE_AGENT);
    }
    {
      unsigned onelo = 0x3f80u; OPAQUE(onelo);
      float c[8];
#pragma unroll
      for (int e = 0; e < 8; ++e) c[e] = 0.f;
#pragma unroll
      for (int j = 0; j < 7; ++j) {
        asm volatile("s_waitcnt vmcnt(%1)" : "+v"(wv[j]) : "i"(7 - j) : "memory");
#pragma unroll
        for (int e = 0; e < 4; ++e) { c[2 * e] = dot2(wv[j][e], onelo, c[2 * e]); c[2 * e + 1] = dot2(wv[j][e], ONE_HI, c[2 * e + 1]); }
        PIN8(c);
      }
#pragma unroll
      for (int e = 0; e < 4; ++e) { auto t = __builtin_amdgcn_permlane32_swap(__float_as_uint(c[e]), __float_as_uint(c[4 + e]), false, false); c[e] = __uint_as_float(t[0]) + __uint_as_float(t[1]); }
#pragma unroll
      for (int e = 0; e < 2; ++e) { auto t = __builtin_amdgcn_permlane16_swap(__float_as_uint(c[e]), __float_as_uint(c[2 + e]), false, false); c[e] = __uint_as_float(t[0]) + __uint_as_float(t[1]); }
      c[0] += ROR(c[0], 8); c[1] += ROR(c[1], 8);
      *(LAS f32x2*)(w * 256 + (lane & 7) * 32 + (lane >> 4) * 8) = f32x2{c[0], c[1]};
      BAR();
      if (w == 0 && ci < 14) {
        float t = 0.f;
#pragma unroll
        for (int j = 0; j < 8; ++j) t += *(const LAS float*)(j * 256 + lane * 4);
        __hip_atomic_store((GAS u64*)(ws + WS_CSG + (64 * cb + lane) * 8), ((u64)(unsigned)tag << 32) | __float_as_uint(t), __ATOMIC_RELAXED, __HIP_MEMORY_SCOPE_AGENT);
      }
    }
    if (a == 31) {
#pragma unroll 1
      for (int j = 0; j < 4; ++j) poll_gran(ws, WS_STG + ((w * 4 + j) * 64 + lane) * 8, (unsigned)tag);
#pragma unroll 1
      for (unsigned spins = 0; spins < SPIN_CAP; ++spins) {
        bool ok = true;
#pragma unroll
        for (int j = 0; j < 4; ++j) {
          const unsigned i = (w * 4 + j) * 64 + lane;
          ok &= __hip_atomic_load((GAS u64*)(ws + WS_HF + (i < 1792 ? i : 0) * FSTR), __ATOMIC_RELAXED, __HIP_MEMORY_SCOPE_AGENT) == tag;
        }
        if (__all(ok)) break;
        __builtin_amdgcn_s_sleep(8);
      }
      BAR();
      if (w == 0) flag_store(ws, WS_EPOCH, tag, lane);
    }
    return;
  }
  const int mt128 = 4 * (g >> 1) + zz, ft = 7 * (g & 1) + yy, hsel = g & 1;
  const int mt64 = 2 * mt128 + hsel, nt = yy;
  const int fg = w & 3, tg = w >> 2;
  const int cg = yy, tt = g + 8 * zz;
  const int ch0 = 64 * cg + 2 * tt;

  const i32x4 sX = mksrd(gx, S_ * D_ * 2), sW1 = mksrd(gw1, D_ * F_ * 2);
  unsigned vx0, vx1, vw0, vw1;
  {
    const int row = 16 * w + (lane >> 3);
    vx0 = (128 * mt128 + row) * 896 + (((lane ^ (lane >> 4)) & 7) << 4);
    vx1 = (vx0 + 8 * 896) ^ 64;
    const int q = lane >> 4;
    vw0 = (8 * w + q) * 3584 + ft * 256 + (((lane & 15) ^ ((q << 2) | (2 * (w & 1)))) << 4);
    vw1 = (vw0 + 4 * 3584) ^ 16;
  }
  const unsigned wb = w * 2048, wb1 = w * 1024;
#define SLX(k) ((k) == 3 ? L_E + 16384 : L_X + ((k) & 3) * 16384)
#define SLW(k) ((k) == 3 ? L_E : L_W + ((k) & 3) * 16384)
  const int rot1 = ROT1 ? (yy + 2 * zz >= 7 ? yy + 2 * zz - 7 : yy + 2 * zz) : 0;
  auto kc = [&](int k) { const int q = k + rot1; return (unsigned)(q >= 7 ? q - 7 : q); };
#define DMA1(k, j) do { \
    if ((j) == 0) dma<SLX(k), 0>(wb, vx0, sX, kc(k) * 128); \
    if ((j) == 1) dma<SLW(k), 0>(wb, vw0, sW1, kc(k) * (64 * 3584)); \
    if ((j) == 2) dma<SLX(k) + 1024, 0>(wb, vx1, sX, kc(k) * 128); \
    if ((j) == 3) dma<SLW(k) + 1024, 0>(wb, vw1, sW1, kc(k) * (64 * 3584)); } while (0)
  DMA1(0, 0); DMA1(0, 1); DMA1(0, 2); DMA1(0, 3);
  const unsigned vr8 = (lane >> 3) * 896 + (lane & 7) * 16;
  dma<L_SW, 0>(wb1, vr8, mksrd(gw2, tt < 28 ? D_ * F_ * 2 : 0), (64 * tt + 8 * w) * 896 + cg * 128);
  __builtin_amdgcn_s_barrier();
  {
    const unsigned kvso = 64 * tt * 896 + cg * 128;
    const i32x4 sK = mksrd(gk, S_ * D_ * 2);
    if (w == 4) dma<L_SK, 0>(0, tt ? vr8 : 0x7fffffffu, sK, tt ? kvso - 8 * 896 : 0);
    if (w == 5) dma<L_TP, 0>(0, lane * 16, mksrd(gshk, D_ * 8), cg * 512);
    dma<L_SK + 1024, 0>(wb1, vr8, sK, kvso + 8 * w * 896);
    dma<L_SV, 0>(wb1, vr8, mksrd(gv, S_ * D_ * 2), kvso + 8 * w * 896);
  }
  DMA1(1, 0); DMA1(1, 1); DMA1(1, 2); DMA1(1, 3);
  const auto rWS = __builtin_amdgcn_make_buffer_rsrc(ws, 0, WS_END, 0x00020000);
  unsigned dpair;
  asm volatile("s_load_dwordx2 %0, %2, 0x0\n\ts_load_dword %1, %3, 0x0" : "=&s"(tag), "=&s"(dpair) : "s"(ws), "s"(gsD + ch0) : "memory");

  unsigned aA0, aA1, aB[4];
  const int blk = (lane >> 4) & 1, q4 = (lane & 15) >> 2, p4 = lane & 3;
  {
    const int ch = 4 * fg + 2 * blk + (p4 >> 1);
    aA0 = L_W + 256 * (8 * hh + q4) + 16 * (ch ^ ((q4 << 2) | (2 * hh))) + 8 * (p4 & 1);
    aA1 = (aA0 + 1024) ^ 16;
    const unsigned b0 = L_X + (64 * tg + r) * 128 + ((hh ^ ((r >> 1) & 7)) << 4);
#pragma unroll
    for (int j = 0; j < 4; ++j) { aB[j] = b0 ^ (j << 5); OPAQUE(aB[j]); }
    OPAQUE(aA0); OPAQUE(aA1);
  }
  f32x16 acc0, acc1;
#pragma unroll
  for (int i = 0; i < 16; ++i) { acc0[i] = 0.f; acc1[i] = 0.f; }
  u32x4 fA[2], fB0[2], fB1[2];
  auto ldf = [&](int n) {
    const int j = n & 3, k = n >> 2;
    unsigned pa0 = aA0, pa1 = aA1, pb = aB[j];
    if (k == 3) { pa0 += L_E - L_W; pa1 += L_E - L_W; pb += L_E + 16384 - L_X; OPAQUE(pa0); OPAQUE(pa1); OPAQUE(pb); }
    const unsigned so = k == 3 ? 0 : (k & 3) * 16384;
    u32x2 a0 = ldstr(pa0 + so + j * 4096), a1 = ldstr(pa1 + so + j * 4096);
    fA[n & 1] = u32x4{a0[0], a0[1], a1[0], a1[1]};
    fB0[n & 1] = lds128(pb + so); fB1[n & 1] = lds128(pb + so + 4096);
  };
#define STEP(n) do { \
    SB(); acc0 = mfma(fA[(n) & 1], fB0[(n) & 1], acc0); asm volatile("" : "+v"(acc0)); SB(); \
    if ((n) + 1 < 28) ldf((n) + 1); \
    SB(); acc1 = mfma(fA[(n) & 1], fB1[(n) & 1], acc1); asm volatile("" : "+v"(acc1)); SB(); \
  } while (0)
#define PIECE(m) DMA1(((m) >> 2) + 2, (m) & 3)
  unsigned kf_so = WS_KVF + ((cg * 32 + tt) * 8 + w) * FSTR, hf_so = WS_HF + ((2 * mt128 + tg) * 56 + 4 * ft + fg) * FSTR, yf_so = WS_YF + ((cg * 8 + w) * 32 + tt) * FSTR;
  i32x4 sH = mksrd(gssk, S_ * D_ * 2);
  unsigned h_so = ch0 * 4096 + wb1, w2t_so = tt < 28 ? WS_W2T + (cg * 112 + 4 * tt) * 2048 + w * 1024 : WS_END;
  asm volatile("" : "+s"(kf_so), "+s"(hf_so), "+s"(yf_so), "+s"(sH), "+s"(h_so), "+s"(w2t_so));
  unsigned kvf_off = WS_KVF + (cg * 256 + (8 * fg + (lane & 7)) * 8 + (tt >> 2)) * FSTR;
  unsigned w2f_off = WS_W2F + (cg * 256 + 28 * w + (lane < 28 ? lane : 0)) * FSTR;
  unsigned gs_off = WS_STG + (128 * mt128 + 64 * tg + lane) * 8, gc_off = WS_CSG + (128 * ft + 32 * fg + r) * 8, b1_off = (128 * ft + 32 * fg + r) * 2;
  unsigned ks_off = (64 * cg + 8 * w + (lane >> 3)) * 4096 + (lane & 7) * 16, ko_rd = L_KO + (8 * w + (lane >> 3)) * 128 + (((lane ^ (lane >> 3)) & 7) << 4);
  unsigned l16 = lane * 16;
  asm volatile("" : "+v"(kvf_off), "+v"(w2f_off), "+v"(gs_off), "+v"(gc_off), "+v"(b1_off), "+v"(ks_off), "+v"(ko_rd), "+v"(l16));
  STAMP(1);
  asm volatile("s_waitcnt vmcnt(6) lgkmcnt(0)\n\ts_barrier" : "+s"(tag), "+s"(dpair) :: "memory");
  tag += 1ull;
  u32x2 tagv = {(unsigned)tag, (unsigned)(tag >> 32)};
  OPAQUE(tagv);
  STAMP(2);
#define TRYF(SENT, SO, Y, FORCE) do { if (!(SENT)) { const unsigned ib = __builtin_amdgcn_s_getreg(7 | (31 << 11)); \
      if ((FORCE) || ((ib & 15u) | ((ib >> 18) & 48u)) <= (Y)) { VMWAIT(Y); flag_put(sWS, SO, tagv); SENT = true; } } } while (0)
  bool wsent = false, ksent = false;
  {
    ldf(0);
    const unsigned wa = L_SW + (16 * (w >> 1) + 4 * hh + q4) * 128 + (w & 1) * 64 + blk * 32 + p4 * 8;
    const u32x2 wa0 = ldstr(wa), wa1 = ldstr(wa + 1024);
    STEP(0);
    st128_sc1(u32x4{wa0[0], wa0[1], wa1[0], wa1[1]}, rWS, l16, w2t_so);
  }
  PIECE(0); STEP(1); PIECE(1); STEP(2); PIECE(2);
  VMWAIT(4); BAR();
  STAMP(3);
  {
    const unsigned ta = (8 * w + q4) * 128 + (lane >> 4) * 32 + p4 * 8;
    u32x2 k0 = ldstr(L_SK + 512 + ta), k1 = ldstr(L_SK + 1024 + ta), k2 = ldstr(L_SK + 1536 + ta);
    u32x2 v0 = ldstr(L_SV + ta), v1 = ldstr(L_SV + 512 + ta);
    u32x2 shk = lds64(L_TP + lane * 8);
    STEP(3);
    PIECE(3);
    BAR();
    PIECE(4);
    TRYF(wsent, kf_so + (WS_W2F - WS_KVF), 5, false);
    const float kr[11] = {bfhi(k0[0]), bflo(k0[1]), bfhi(k0[1]), bflo(k1[0]), bfhi(k1[0]), bflo(k1[1]), bfhi(k1[1]), bflo(k2[0]), bfhi(k2[0]), bflo(k2[1]), bfhi(k2[1])};
    const float tw0 = bflo(shk[0]), tw1 = bfhi(shk[0]), tw2 = bflo(shk[1]), tw3 = bfhi(shk[1]);
    float a[8]; unsigned pp[8];
#pragma unroll
    for (int j = 0; j < 8; ++j) pp[j] = pk(fmul1(kr[j], tw3), fmul1(kr[j + 1], tw2));
    PIECE(5);
    dot2x8(a, pp, ONES, ONES, ONES, ONES);
#pragma unroll
    for (int j = 0; j < 8; ++j) pp[j] = pk(a[j], fmul1(kr[j + 2], tw1));
    PIECE(6);
    dot2x8(a, pp, ONES, ONES, ONES, ONES);
#pragma unroll
    for (int j = 0; j < 8; ++j) pp[j] = pk(a[j], fmul1(kr[j + 3], tw0));
    PIECE(7);
    dot2x8(a, pp, ONES, ONES, ONES, ONES);
#pragma unroll
    for (int j = 0; j < 8; j += 2) { pp[j] = pk(a[j], 0.f); pp[j + 1] = pk(0.f, a[j + 1]); }
    dot2x8(a, pp, v0[0], v0[1], v1[0], v1[1]);
    sts128(L_KO + lane * 128 + ((w ^ (lane & 7)) << 4), u32x4{pk(a[7], a[6]), pk(a[5], a[4]), pk(a[3], a[2]), pk(a[1], a[0])});
    sts128(L_U + (fg >> 1) * U_SZ + ((fg & 1) * 2 + hh) * U_PL + r * 16, u32x4{0u, 0u, 0u, 0u});
    TRYF(wsent, kf_so + (WS_W2F - WS_KVF), 8, false);
    SB();
  }
  STAMP(20);
  STEP(4); PIECE(8); STEP(5); PIECE(9); STEP(6); PIECE(10);
  VMWAIT(7); TRYF(wsent, kf_so + (WS_W2F - WS_KVF), 11, true); BAR();
  STAMP(4);
  {
    const u32x4 o = lds128(ko_rd);
    STEP(7);
    st128_sc1(o, rWS, ks_off, WS_KVT + tt * 128);
  }
  PIECE(11); STEP(8); PIECE(12); STEP(9); PIECE(13); STEP(10); PIECE(14);
  VMWAIT(8); TRYF(ksent, kf_so, 4, false); BAR();
  STAMP(5);
  u32x2 pf, pw, gs, gc; unsigned b1v = 0; u32x4 uld;
  STEP(11); PIECE(15); STEP(12);
  TRYF(ksent, kf_so, 5, false);
  asm volatile("buffer_load_dwordx2 %0, %1, %2, 0 offen sc1" : "=v"(pw) : "v"(w2f_off), "s"(sWS) : "memory");
  PIECE(16); STEP(13); PIECE(17); STEP(14); PIECE(18);
  VMWAIT(8); TRYF(ksent, kf_so, 9, true);
  BAR();
  STAMP(6);
  u32x4 wf[28];
  const unsigned w2so = WS_W2T + (nt * 112 + 14 * w) * 2048;
  auto ldw2 = [&](int n0, int n1) {
#pragma unroll
    for (int n = n0; n < n1; ++n) { wf[n] = __builtin_amdgcn_raw_buffer_load_b128(rWS, l16 + (n & 3) * 1024, w2so + (n >> 2) * 4096, 16); SB(); }
  };
  STEP(15); PIECE(19);
  STEP(16); dma<L_SH, 0>(wb1, l16, sH, h_so);
  asm volatile("s_waitcnt vmcnt(5)" : "+v"(pw) :: "memory");
  if (!__all((((u64)pw[1] << 32) | pw[0]) == tag)) poll_flags(ws, w2f_off, tag);
  STEP(17); ldw2(0, 1);
  STEP(18); ldw2(1, 2);
  BAR();
  STAMP(7);
  STEP(19);
  asm volatile("global_load_short_d16_hi %0, %1, %2" : "+v"(b1v) : "v"(b1_off), "s"(gb1) : "memory");
  asm volatile("buffer_load_dwordx2 %0, %1, %2, 0 offen sc1" : "=v"(gs) : "v"(gs_off), "s"(sWS) : "memory");
  asm volatile("buffer_load_dwordx2 %0, %1, %2, 0 offen sc1" : "=v"(gc) : "v"(gc_off), "s"(sWS) : "memory");
  STEP(20); ldw2(2, 3); STEP(21); ldw2(3, 4); STEP(22); ldw2(4, 5);
  VMWAIT(8);
  BAR();
  STAMP(8);
  STEP(23); ldw2(5, 6);
  unsigned ed[4][3];
  {
    unsigned ra = L_SH + w * 1024 + lane * 4;
    OPAQUE(ra);
#pragma unroll
    for (int j = 0; j < 4; ++j)
#pragma unroll
      for (int e = 0; e < 3; ++e) ed[j][e] = *(const LAS unsigned*)(ra + j * 256 + e * 4);
  }
  STEP(24);
  asm volatile("buffer_load_dwordx2 %0, %1, %2, 0 offen sc1" : "=v"(pf) : "v"(kvf_off), "s"(sWS) : "memory");
  STEP(25); ldw2(6, 7);
  STEP(26); ldw2(7, 8);
  STEP(27);
  asm volatile("s_waitcnt vmcnt(7)" : "+v"(b1v), "+v"(gs), "+v"(gc) :: "memory");
  if (!__all(gs[1] == (unsigned)tag && gc[1] == (unsigned)tag)) { gs[0] = poll_gran(ws, gs_off, (unsigned)tag); gc[0] = poll_gran(ws, gc_off, (unsigned)tag); }
  STAMP(10);
  const unsigned u_so = WS_KVT + (ch0 + tg) * 4096 + fg * 1024;
  {
    if (fg == 0) *(LAS unsigned*)(L_ST + (64 * tg + lane) * 4) = gs[0];
    const unsigned nm = gs[0] ^ 0x8000u;
    const unsigned bm = __builtin_amdgcn_perm(nm, nm, 0x01000100u);
    const float c = __uint_as_float(gc[0]), chi = rbf(c), c1 = c - chi, cmid = rbf(c1);
    const u32x4 A = {pk(chi, cmid), pk(c1 - cmid, __uint_as_float(b1v)), 0u, 0u};
    acc0 = mfma(A, u32x4{hh ? 0u : bm, hh ? 0u : nm, 0u, 0u}, acc0);
    acc1 = mfma(A, u32x4{hh ? bm : 0u, hh ? nm : 0u, 0u, 0u}, acc1);
    asm volatile("s_waitcnt vmcnt(2)" : "+v"(pf) :: "memory");
    asm volatile("buffer_load_dwordx4 %0, %1, %2, %3 offen sc1" : "=v"(uld) : "v"(l16), "s"(sWS), "s"(u_so) : "memory");
    ldw2(8, 10);
    const unsigned so = WS_HT + ((4 * mt128 + 2 * tg) * 112 + 8 * ft + 2 * fg) * 1024;
    unsigned wa = L_E + 256 + tg * E_SZ + fg * 4096 + lane * 16;
    OPAQUE(wa);
#pragma unroll
    for (int q = 0; q < 4; ++q) {
      sts128(wa + q * 1024, u32x4{ed[q][0], ed[q][1], __builtin_amdgcn_alignbit(ed[q][1], ed[q][0], 16), __builtin_amdgcn_alignbit(ed[q][2], ed[q][1], 16)});
      u32x4 hv;
#pragma unroll
      for (int e = 0; e < 4; ++e) {
        float a0 = (q < 2 ? acc0 : acc1)[8 * (q & 1) + 2 * e], a1 = (q < 2 ? acc0 : acc1)[8 * (q & 1) + 2 * e + 1];
        a0 *= __builtin_fabsf(a0); OPAQUE(a0);
        a1 *= __builtin_fabsf(a1);
        const s16x2 m = __builtin_elementwise_max(__builtin_bit_cast(s16x2, pk(a0, a1)), s16x2{0, 0});
        hv[e] = __builtin_bit_cast(unsigned, m);
      }
      st128_sc1(hv, rWS, (ABL & 8) ? 0x7fffffffu : l16 + (q & 1) * 1024, so + (q >> 1) * 112 * 1024);
    }
  }
  STAMP(11);
  asm volatile("s_waitcnt vmcnt(6)" : "+v"(uld) :: "memory");
  if (!__all((((u64)pf[1] << 32) | pf[0]) == tag)) {
    poll_flags(ws, kvf_off, tag);
    asm volatile("buffer_load_dwordx4 %0, %1, %2, %3 offen sc1\n\ts_waitcnt vmcnt(0)" : "=&v"(uld) : "v"(l16), "s"(sWS), "s"(u_so) : "memory");
  }
  {
    const int n = 64 * fg + lane;
    sts128(L_U + tg * U_SZ + (n & 3) * U_PL + ((n >> 2) + 32) * 16, uld);
    if (fg == 0) {
      const unsigned h0 = __builtin_amdgcn_readfirstlane(ed[0][0]), h1 = __builtin_amdgcn_readfirstlane(__builtin_amdgcn_alignbit(ed[0][1], ed[0][0], 16));
      const int l = lane & 15;
      sts128(L_E + tg * E_SZ + l * 16, u32x4{0u, l == 15 ? h0 : 0u, l == 15 ? h0 << 16 : 0u, l == 15 ? h1 : l == 14 ? h0 << 16 : 0u});
    }
  }
  ldw2(10, 12);
  BAR();
  STAMP(21);
  const int tok = threadIdx.x >> 4, quad = threadIdx.x & 15;
  const unsigned hf_off = WS_HF + (mt64 * 56 + 7 * w + (lane < 7 ? lane : 6)) * FSTR;
  const unsigned xo = ((64 * mt64 + tok) * D_ + 64 * nt + 4 * quad) * 2;
  u32x2 hf, xr0, xr1, b2r;
  constexpr int WIN = HWIN, NH0 = (16 - HCHK) * HPS;
  u32x4 hb[WIN];
  const unsigned hso = WS_HT + (2 * mt64 * 112 + 14 * w) * 1024;
  auto ldh = [&](int n) {
    const int mt = n / 14, ks = n % 14;
    hb[n % WIN] = __builtin_amdgcn_raw_buffer_load_b128(rWS, l16, hso + mt * 112 * 1024 + ks * 1024, 16);
    SB();
  };
  {
    f32x16 c0, c1;
#pragma unroll
    for (int i = 0; i < 16; ++i) { c0[i] = 0.f; c1[i] = 0.f; }
    unsigned ea0 = L_E + tg * E_SZ + 8 * (32 + r - 8 * hh - 7 + 256 * fg - 16), ea1 = ea0 + 32, ea2 = ea0 + 8192, ea3 = ea0 + 8224;
    unsigned ua = L_U + tg * U_SZ + hh * U_PL + 16 * (r + 32 - 8 * fg - 7);
    OPAQUE(ea0); OPAQUE(ea1); OPAQUE(ea2); OPAQUE(ea3); OPAQUE(ua);
    u32x4 A0[2], A1[2], B0[2], B1[2];
    auto ldca = [&](int st) {
      const int dd = st >> 1, s = st & 1;
      const unsigned eo = 8 * (32 * dd + 16 * (1 - s)), uo = 2 * s * U_PL + 16 * (7 - dd);
      u32x2 x0 = lds64(ea0 + eo), x1 = lds64(ea1 + eo);
      B0[st & 1] = lds128(ua + uo);
      A0[st & 1] = u32x4{x0[0], x0[1], x1[0], x1[1]};
    };
    auto ldcb = [&](int st) {
      const int dd = st >> 1, s = st & 1;
      const unsigned eo = 8 * (32 * dd + 16 * (1 - s)), uo = 2 * s * U_PL + 16 * (7 - dd);
      B1[st & 1] = lds128(ua + uo + 512);
      u32x2 y0 = lds64(ea2 + eo), y1 = lds64(ea3 + eo);
      A1[st & 1] = u32x4{y0[0], y0[1], y1[0], y1[1]};
    };
    ldca(0); ldcb(0);
    SB();
    bool hsent = false;
#pragma unroll
    for (int st = 0; st < 16; ++st) {
      c0 = mfma(A0[st & 1], B0[st & 1], c0); asm volatile("" : "+v"(c0)); SB();
      if (st + 1 < 16) ldca(st + 1);
      SB();
      c1 = mfma(A0[st & 1], B1[st & 1], c1); asm volatile("" : "+v"(c1)); SB();
      if (st + 1 < 16) ldcb(st + 1);
      SB();
      c1 = mfma(A1[st & 1], B0[st & 1], c1); asm volatile("" : "+v"(c1)); SB();
      if (st >= HF0 && st <= SIGF && (st & 1) && !hsent) {
        const int younger = 2 + (st < 8 ? 2 * st : 16);
        const unsigned ib = __builtin_amdgcn_s_getreg(7 | (31 << 11));
        if (st == SIGF || ((ib & 15u) | ((ib >> 18) & 48u)) <= (unsigned)younger) {
          VMWAIT(younger);
          flag_put(sWS, hf_so, tagv);
          hsent = true;
        }
      }
      if (st < 8) ldw2(12 + 2 * st, 14 + 2 * st);
      if (st == 8) {
        asm volatile("buffer_load_dwordx2 %0, %1, %2, 0 offen" : "=v"(xr0) : "v"(xo), "s"(sX) : "memory");
        asm volatile("buffer_load_dwordx2 %0, %1, %2, %3 offen" : "=v"(xr1) : "v"(xo), "s"(sX), "s"(32 * 896) : "memory");
        asm volatile("global_load_dwordx2 %0, %1, %2" : "=v"(b2r) : "v"((64 * nt + 4 * quad) * 2), "s"(gb2) : "memory");
      }
      if (st == HPOLL) asm volatile("buffer_load_dwordx2 %0, %1, %2, 0 offen sc1" : "=v"(hf) : "v"(hf_off), "s"(sWS) : "memory");
      if (st == HCHK) {
        STAMP(9);
        asm volatile("s_waitcnt vmcnt(0)" : "+v"(hf), "+v"(xr0), "+v"(xr1), "+v"(b2r) :: "memory");
        if (!__all((((u64)hf[1] << 32) | hf[0]) == tag)) poll_flags(ws, hf_off, tag);
        STAMP(22);
      }
      if (st >= HCHK) {
#pragma unroll
        for (int n = (st - HCHK) * HPS; n < (st - HCHK + 1) * HPS; ++n) ldh(n);
      }
    }
    STAMP(12);
    const unsigned pb = L_CP + w * P_SZ + lane * 16;
#pragma unroll
    for (int g4 = 0; g4 < 4; ++g4) {
      *(LAS f32x4*)(pb + g4 * 1088) = f32x4{c0[4 * g4], c0[4 * g4 + 1], c0[4 * g4 + 2], c0[4 * g4 + 3]};
      *(LAS f32x4*)(pb + 4352 + g4 * 1088) = f32x4{c1[4 * g4], c1[4 * g4 + 1], c1[4 * g4 + 2], c1[4 * g4 + 3]};
    }
  }
  BAR();
  STAMP(13);
  {
    const int n = 32 * w + r, a = n >> 2, g4 = n & 3;
    const unsigned po = L_CP + hh * 4 * P_SZ + (a >> 5) * 4352 + g4 * 1088 + (a & 31) * 16;
    f32x4 lo = *(const LAS f32x4*)po, hi = *(const LAS f32x4*)(po + 512);
#pragma unroll
    for (int q = 1; q < 4; ++q) {
      lo += *(const LAS f32x4*)(po + q * P_SZ);
      hi += *(const LAS f32x4*)(po + q * P_SZ + 512);
    }
    const u32x4 kvc = lds128(L_U + hh * U_SZ + g4 * U_PL + (a + 32) * 16);
    const unsigned dlo = hh ? dpair >> 16 : dpair & 0xffffu, dhi = dlo << 16;
    float pd[8], yv[8];
    { const unsigned kk[8] = {kvc[3], kvc[2], kvc[1], kvc[0], kvc[3], kvc[2], kvc[1], kvc[0]}; dot2x8(pd, kk, dhi, dhi, dlo, dlo); }
    { const unsigned pp[8] = {pk(lo[0], pd[0]), pk(lo[1], pd[4]), pk(lo[2], pd[1]), pk(lo[3], pd[5]), pk(hi[0], pd[2]), pk(hi[1], pd[6]), pk(hi[2], pd[3]), pk(hi[3], pd[7])};
      dot2x8(yv, pp, ONES, ONES, ONES, ONES); }
    const u32x4 yo = {pk(yv[0], yv[1]), pk(yv[2], yv[3]), pk(yv[4], yv[5]), pk(yv[6], yv[7])};
    st128_sc1(yo, rWS, (ch0 + hh) * 4096 + n * 16, WS_YT);
  }
  STAMP(14);
#pragma unroll
  for (int n = NH0; n < WIN; ++n) ldh(n);
  auto gate = [&](unsigned ra, float r2, unsigned (&gt)[2]) {
    f32x4 sm = *(const LAS f32x4*)ra;
#pragma unroll
    for (int q = 1; q < 8; ++q) sm += *(const LAS f32x4*)(ra + q * G_SZ);
    const unsigned oa = pk(fmaf(sm[0], r2, bflo(b2r[0])), fmaf(sm[1], r2, bfhi(b2r[0]))), ob = pk(fmaf(sm[2], r2, bflo(b2r[1])), fmaf(sm[3], r2, bfhi(b2r[1])));
    const float o[4] = {bflo(oa), bfhi(oa), bflo(ob), bfhi(ob)};
    float sv[4];
#pragma unroll
    for (int e = 0; e < 4; ++e) sv[e] = o[e] * __builtin_amdgcn_rcpf(1.f + __builtin_amdgcn_exp2f(o[e] * -1.4426950408889634f));
    gt[0] = pk(sv[0], sv[1]); gt[1] = pk(sv[2], sv[3]);
  };
  auto xnorm = [&](u32x2 xr, unsigned st, unsigned (&xn)[2], float& r2, float* xf) {
    const float m = bflo(st), rd = __builtin_amdgcn_rcpf(bfhi(st));
    xf[0] = bflo(xr[0]); xf[1] = bfhi(xr[0]); xf[2] = bflo(xr[1]); xf[3] = bfhi(xr[1]);
    const unsigned da = pk(xf[0] - m, xf[1] - m), db = pk(xf[2] - m, xf[3] - m);
    xn[0] = pk(bflo(da) * rd, bfhi(da) * rd); xn[1] = pk(bflo(db) * rd, bfhi(db) * rd);
    r2 = rd * rd;
  };
  unsigned g0[2], g1[2], xn0[2], xn1[2];
  float r20, r21, xf[8];
  unsigned rp0 = L_G0 + quad * G_H + tok * 16, rp1 = rp0 - (L_G0 - L_G1), yra = L_Y + 4 * quad * 132 + tok * 2;
  const unsigned yf_off = WS_YF + ((nt * 8 + (mt64 >> 2)) * 32 + 4 * w + (lane & 3)) * FSTR;
  u32x2 yf; u32x4 yb;
  const unsigned yvo = (lane >> 3) * 4096 + (lane & 7) * 16, yso = WS_YT + (64 * nt + 8 * w) * 4096 + mt64 * 128;
  STAMP(15);
  {
    f32x16 ca[2], cb[2];
    f32x4 gp[4], gq[4]; float gs4[4], ge[4];
    const float nl2e = -1.4426950408889634f;
#pragma unroll
    for (int i = 0; i < 16; ++i) { ca[0][i] = 0.f; cb[0][i] = 0.f; ca[1][i] = 0.f; cb[1][i] = 0.f; }
    unsigned pb0 = L_G0 + w * G_SZ + hh * G_H + r * 16, pb1 = pb0 - (L_G0 - L_G1);
    OPAQUE(pb0); OPAQUE(pb1);
    auto wrp = [&](unsigned pb, const f32x16& c, int g4, int t) { *(LAS f32x4*)(pb + t * G_T + g4 * G_G) = f32x4{c[4 * g4], c[4 * g4 + 1], c[4 * g4 + 2], c[4 * g4 + 3]}; };
#pragma unroll
    for (int n = 0; n < 28; ++n) {
      const int mt = n / 14, ks = n % 14;
      if (n == NH0) { VMWAIT(WIN - 1); flag_put(sWS, yf_so, tagv); }
      ca[mt] = mfma(wf[2 * ks], hb[n % WIN], ca[mt]); asm volatile("" : "+v"(ca[mt])); SB();
      if (n >= 1 && n - 1 + WIN < 28) ldh(n - 1 + WIN);
      if (n >= 14 && n < 18) { wrp(pb0, ca[0], n - 14, 0); SB(); }
      cb[mt] = mfma(wf[2 * ks + 1], hb[n % WIN], cb[mt]); asm volatile("" : "+v"(cb[mt])); SB();
      if (n == 1) {
        const unsigned st0 = *(const LAS unsigned*)(L_ST + (64 * hsel + tok) * 4), st1 = *(const LAS unsigned*)(L_ST + (64 * hsel + 32 + tok) * 4);
        xnorm(xr0, st0, xn0, r20, xf); xnorm(xr1, st1, xn1, r21, xf + 4);
        asm volatile("" : "+v"(xn0[0]), "+v"(xn0[1]), "+v"(xn1[0]), "+v"(xn1[1]), "+v"(r20), "+v"(r21), "+v"(rp0), "+v"(rp1), "+v"(yra)); PIN8(xf); SB();
      }
      if (n >= 14 && n < 18) { wrp(pb0, cb[0], n - 14, 1); SB(); }
      if (n == 20) { BAR(); STAMP(16); }
      if (n == 20 || n == 21) {
#pragma unroll
        for (int q = 0; q < 4; ++q) gp[q] = *(const LAS f32x4*)(rp0 + (4 * (n - 20) + q) * G_SZ);
        SB();
      }
      if (n == 21 || n == 22) {
#pragma unroll
        for (int e = 0; e < 4; ++e) { const float t = fadd1(fadd1(gq[0][e], gq[1][e]), fadd1(gq[2][e], gq[3][e])); gs4[e] = n == 21 ? t : fadd1(gs4[e], t); }
        asm volatile("" : "+v"(gs4[0]), "+v"(gs4[1]), "+v"(gs4[2]), "+v"(gs4[3])); SB();
      }
      if (n == 20 || n == 21) {
#pragma unroll
        for (int q = 0; q < 4; ++q) gq[q] = gp[q];
      }
      if (n == 23) {
        const unsigned oa = pk(ffma1(gs4[0], r20, bflo(b2r[0])), ffma1(gs4[1], r20, bfhi(b2r[0]))), ob = pk(ffma1(gs4[2], r20, bflo(b2r[1])), ffma1(gs4[3], r20, bfhi(b2r[1])));
        gs4[0] = bflo(oa); gs4[1] = bfhi(oa); gs4[2] = bflo(ob); gs4[3] = bfhi(ob);
        asm volatile("" : "+v"(gs4[0]), "+v"(gs4[1]), "+v"(gs4[2]), "+v"(gs4[3])); SB();
      }
      if (n == 24) {
#pragma unroll
        for (int e = 0; e < 4; ++e) ge[e] = __builtin_amdgcn_exp2f(fmul1(gs4[e], nl2e));
        asm volatile("" : "+v"(ge[0]), "+v"(ge[1]), "+v"(ge[2]), "+v"(ge[3])); SB();
      }
      if (n == 25) {
#pragma unroll
        for (int e = 0; e < 4; ++e) ge[e] = __builtin_amdgcn_rcpf(1.f + ge[e]);
        asm volatile("" : "+v"(ge[0]), "+v"(ge[1]), "+v"(ge[2]), "+v"(ge[3])); SB();
      }
      if (n == 26) {
        g0[0] = pk(fmul1(gs4[0], ge[0]), fmul1(gs4[1], ge[1])); g0[1] = pk(fmul1(gs4[2], ge[2]), fmul1(gs4[3], ge[3]));
        asm volatile("" : "+v"(g0[0]), "+v"(g0[1])); SB();
      }
      if (n == YPOLL) asm volatile("buffer_load_dwordx2 %0, %1, %2, 0 offen sc1" : "=v"(yf) : "v"(yf_off), "s"(sWS) : "memory");
      if (n == YPOLL + YLAG) {
        asm volatile("s_waitcnt vmcnt(0)" : "+v"(yf) :: "memory");
        asm volatile("buffer_load_dwordx4 %0, %1, %2, %3 offen sc1" : "=v"(yb) : "v"(yvo), "s"(sWS), "s"(yso) : "memory");
      }
    }
#pragma unroll
    for (int g4 = 0; g4 < 4; ++g4) { wrp(pb1, ca[1], g4, 0); wrp(pb1, cb[1], g4, 1); }
  }
  STAMP(17);
  asm volatile("s_waitcnt vmcnt(0)" : "+v"(yb) :: "memory");
  if (!__all((((u64)yf[1] << 32) | yf[0]) == tag)) {
    poll_flags(ws, yf_off, tag);
    asm volatile("buffer_load_dwordx4 %0, %1, %2, %3 offen sc1\n\ts_waitcnt vmcnt(0)" : "=&v"(yb) : "v"(yvo), "s"(sWS), "s"(yso) : "memory");
  }
  {
    const unsigned ya = L_Y + (8 * w + (lane >> 3)) * 132 + (lane & 7) * 16;
#pragma unroll
    for (int e = 0; e < 4; ++e) *(LAS unsigned*)(ya + 4 * e) = yb[e];
  }
  BAR();
  STAMP(18);
  {
    unsigned yv[8];
#pragma unroll
    for (int e = 0; e < 8; ++e) yv[e] = *(const LAS u16*)(yra + (e & 3) * 132 + (e >> 2) * 64);
    gate(rp1, r21, g1);
    float t[8]; unsigned pp[8];
#pragma unroll
    for (int e = 1; e < 8; e += 2) yv[e] <<= 16;
    dot2x8(t, yv, xn0[0], xn0[1], xn1[0], xn1[1]);
#pragma unroll
    for (int e = 0; e < 8; e += 2) { pp[e] = pk(t[e], 0.f); pp[e + 1] = pk(0.f, t[e + 1]); }
    dot2x8(t, pp, g0[0], g0[1], g1[0], g1[1]);
#pragma unroll
    for (int e = 0; e < 8; ++e) pp[e] = pk(t[e], xf[e]);
    dot2x8(t, pp, ONES, ONES, ONES, ONES);
    const auto rO = __builtin_amdgcn_make_buffer_rsrc(gout, 0, S_ * D_ * 2, 0x00020000);
    __builtin_amdgcn_raw_buffer_store_b64(u32x2{pk(t[0], t[1]), pk(t[2], t[3])}, rO, xo, 0, OUTAUX);
    __builtin_amdgcn_raw_buffer_store_b64(u32x2{pk(t[4], t[5]), pk(t[6], t[7])}, rO, xo, 32 * 896, OUTAUX);
  }
}

extern "C" void kernel_launch(void* const* d_in, const int* in_sizes, int n_in, void* d_out, int out_size, void* d_ws, size_t ws_size,
                              hipStream_t stream) {
  (void)in_sizes; (void)n_in; (void)out_size; (void)ws_size;
  (void)hipFuncSetAttribute((const void*)fused_h3_mlp, hipFuncAttributeMaxDynamicSharedMemorySize, LDS_BYTES);
  fused_h3_mlp<<<dim3(8, 8, 4), 512, LDS_BYTES, stream>>>((const u16*)d_in[0], (const u16*)d_in[1], (const u16*)d_in[2], (const u16*)d_in[3],
                                                         (const u16*)d_in[4], (const u16*)d_in[5], (const u16*)d_in[7], (const u16*)d_in[8],
                                                         (const u16*)d_in[9], (const u16*)d_in[10], (u16*)d_out, (char*)d_ws);
}
```

```cpp
#include <hip/hip_runtime.h>
#include <hip/hip_bf16.h>
#include <stdint.h>
#include <type_traits>

typedef unsigned short u16;
typedef unsigned long long u64;
typedef __bf16 bf16x8 __attribute__((ext_vector_type(8)));
typedef __bf16 bf16x2 __attribute__((ext_vector_type(2)));
typedef short s16x2 __attribute__((ext_vector_type(2)));
typedef short s16x4 __attribute__((ext_vector_type(4)));
typedef float f32x2 __attribute__((ext_vector_type(2)));
typedef float f32x4 __attribute__((ext_vector_type(4)));
typedef float f32x16 __attribute__((ext_vector_type(16)));
typedef unsigned u32x2 __attribute__((ext_vector_type(2)));
typedef unsigned u32x4 __attribute__((ext_vector_type(4)));
typedef int i32x4 __attribute__((ext_vector_type(4)));
#define GAS __attribute__((address_space(1)))
#define LAS __attribute__((address_space(3)))
#define DI __device__ __forceinline__
#pragma clang diagnostic ignored "-Wint-to-pointer-cast"

constexpr int S_ = 2048, D_ = 448, F_ = 1792;
constexpr int NWG = 224;
constexpr unsigned FSTR = 256;
constexpr unsigned WS_EPOCH = 0;
constexpr unsigned WS_KVF = 4096;
constexpr unsigned WS_HF = WS_KVF + 1792 * FSTR;
constexpr unsigned WS_YF = WS_HF + 1792 * FSTR;
constexpr unsigned WS_W2F = WS_YF + 1792 * FSTR;
constexpr unsigned WS_STG = WS_W2F + 1792 * FSTR;
constexpr unsigned WS_CSG = WS_STG + 2048 * 8;
constexpr unsigned WS_KVT = 2u << 20;
constexpr unsigned WS_YT = 4u << 20;
constexpr unsigned WS_HT = 6u << 20;
constexpr unsigned WS_W2T = WS_HT + 64u * 112u * 1024u;
constexpr unsigned WS_END = WS_W2T + 7u * 112u * 2048u;
static_assert(WS_CSG + 1792 * 8 <= WS_KVT, "ws");
constexpr unsigned WS_DBG = 16u << 20;
constexpr unsigned L_X = 49152;
constexpr unsigned L_W = 0;
constexpr unsigned L_E = 98304;
constexpr unsigned E_SZ = 16640;
constexpr unsigned U_PL = 1568, U_SZ = 4 * U_PL;
constexpr unsigned L_SH = L_E + 2 * E_SZ;
constexpr unsigned L_SS = L_SH + 8192 + 64;
constexpr unsigned L_KV = L_SS + 1024;
constexpr unsigned L_Y = L_KV;
constexpr unsigned L_ST = L_KV + 9216;
constexpr unsigned L_U = L_ST + 1024;
constexpr unsigned P_SZ = 8704;
constexpr unsigned L_CP = 0;
constexpr unsigned L_G0 = 69632;
constexpr unsigned G_SZ = 8448;
constexpr unsigned L_G1 = 0;
constexpr unsigned L_SK = L_U;
constexpr unsigned L_TP = L_U + 9216;
constexpr unsigned L_SV = L_KV;
constexpr unsigned L_KO = L_KV;
constexpr unsigned L_SW = L_SH;
constexpr unsigned G_H = 528, G_G = 1056, G_T = 4224;
constexpr unsigned LDS_BYTES = L_U + 2 * U_SZ;
static_assert(L_G0 + 8 * G_SZ <= L_KV && L_U % 16 == 0, "lds");
static_assert(LDS_BYTES <= 163840, "lds");

constexpr unsigned SPIN_CAP = 1u << 18;
constexpr unsigned ONES = 0x3f803f80u, ONE_HI = 0x3f800000u;

DI float bflo(unsigned v) { return __uint_as_float(v << 16); }
DI float bfhi(unsigned v) { return __uint_as_float(v & 0xffff0000u); }
DI unsigned pk(float lo, float hi) { f32x2 f = {lo, hi}; return __builtin_bit_cast(unsigned, __builtin_convertvector(f, bf16x2)); }
DI float fmul1(float a, float b) { float d; asm("v_mul_f32 %0, %1, %2" : "=v"(d) : "v"(a), "v"(b)); return d; }
DI float fadd1(float a, float b) { float d; asm("v_add_f32 %0, %1, %2" : "=v"(d) : "v"(a), "v"(b)); return d; }
DI float ffma1(float a, float b, float c) { float d; asm("v_fma_f32 %0, %1, %2, %3" : "=v"(d) : "v"(a), "v"(b), "v"(c)); return d; }
DI float rbf(float a) { return bfhi(pk(a, a)); }
DI float dot2(unsigned a, unsigned b, float c) { return __builtin_amdgcn_fdot2_f32_bf16(__builtin_bit_cast(bf16x2, a), __builtin_bit_cast(bf16x2, b), c, false); }
DI void dot2x8(float (&d)[8], const unsigned (&a)[8], unsigned b0, unsigned b1, unsigned b2, unsigned b3) {
  asm volatile("v_dot2_f32_bf16 %0, %8, %16, 0\n\tv_dot2_f32_bf16 %1, %9, %16, 0\n\tv_dot2_f32_bf16 %2, %10, %17, 0\n\tv_dot2_f32_bf16 %3, %11, %17, 0\n\t"
               "v_dot2_f32_bf16 %4, %12, %18, 0\n\tv_dot2_f32_bf16 %5, %13, %18, 0\n\tv_dot2_f32_bf16 %6, %14, %19, 0\n\tv_dot2_f32_bf16 %7, %15, %19, 0\n\ts_nop 2"
               : "=&v"(d[0]), "=&v"(d[1]), "=&v"(d[2]), "=&v"(d[3]), "=&v"(d[4]), "=&v"(d[5]), "=&v"(d[6]), "=&v"(d[7])
               : "v"(a[0]), "v"(a[1]), "v"(a[2]), "v"(a[3]), "v"(a[4]), "v"(a[5]), "v"(a[6]), "v"(a[7]), "v"(b0), "v"(b1), "v"(b2), "v"(b3));
}
DI f32x16 mfma(u32x4 a, u32x4 b, f32x16 c) { return __builtin_amdgcn_mfma_f32_32x32x16_bf16(__builtin_bit_cast(bf16x8, a), __builtin_bit_cast(bf16x8, b), c, 0, 0, 0); }
DI u32x4 lds128(unsigned a) { return *(const LAS u32x4*)a; }
DI u32x2 lds64(unsigned a) { return *(const LAS u32x2*)a; }
DI u32x2 ldstr(unsigned a) { return __builtin_bit_cast(u32x2, __builtin_amdgcn_ds_read_tr16_b64_v4i16((LAS s16x4*)a)); }
DI void sts128(unsigned a, u32x4 v) { *(LAS u32x4*)a = v; }
DI i32x4 mksrd(const void* p, unsigned bytes) {
  u64 a = (u64)p;
  i32x4 r = {(int)(unsigned)a, (int)((unsigned)(a >> 32) & 0xffffu), (int)bytes, 0x00020000};
  return r;
}
#define VMWAIT(n) asm volatile("s_waitcnt vmcnt(%0)" ::"i"(n) : "memory")
#define BAR() asm volatile("s_waitcnt lgkmcnt(0)\n\ts_barrier" ::: "memory")
#define OPAQUE(x) asm volatile("" : "+v"(x))
#define SB() __builtin_amdgcn_sched_barrier(0)
#define ROR(x, n) __uint_as_float(__builtin_amdgcn_update_dpp(0u, __float_as_uint(x), 0x120 + (n), 0xf, 0xf, false))
#define PIN8(a) asm volatile("" : "+v"(a[0]), "+v"(a[1]), "+v"(a[2]), "+v"(a[3]), "+v"(a[4]), "+v"(a[5]), "+v"(a[6]), "+v"(a[7]))
template <int LOFF, int GOFF> DI void dma(unsigned wb, unsigned voff, i32x4 srd, unsigned soff) {
  asm volatile("s_add_i32 m0, %0, %4\n\ts_nop 0\n\tbuffer_load_dwordx4 %1, %2, %3 offen offset:%5 lds"
               ::"s"(wb), "v"(voff), "s"(srd), "s"(soff), "i"(LOFF - GOFF), "i"(GOFF) : "scc");
}
template <typename R> DI void st128_sc1(u32x4 v, R rsrc, unsigned voff, unsigned soff) {
  __builtin_amdgcn_raw_buffer_store_b128(v, rsrc, voff, soff, 16);
  asm volatile("s_nop 1" ::"v"(v) : "memory");
}
DI void flag_store(char* ws, unsigned off, u64 tag, int lane) {
  if (lane == 0) __hip_atomic_store((GAS u64*)(ws + off), tag, __ATOMIC_RELAXED, __HIP_MEMORY_SCOPE_AGENT);
}
DI void flag_put(i32x4 srd, unsigned soff, u32x2 tagv) { asm volatile("buffer_store_dwordx2 %0, off, %1, %2 sc1" ::"v"(tagv), "s"(srd), "s"(soff) : "memory"); }
DI void poll_flags(char* ws, unsigned off, u64 tag) {
#pragma unroll 1
  for (unsigned spins = 0; spins < SPIN_CAP; ++spins) {
    u64 f = __hip_atomic_load((GAS u64*)(ws + off), __ATOMIC_RELAXED, __HIP_MEMORY_SCOPE_AGENT);
    if (__all(f == tag)) break;
    __builtin_amdgcn_s_sleep(1);
  }
  asm volatile("" ::: "memory");
}

DI unsigned poll_gran(char* ws, unsigned off, unsigned tag) {
  u64 f = 0;
#pragma unroll 1
  for (unsigned spins = 0; spins < SPIN_CAP; ++spins) {
    f = __hip_atomic_load((GAS u64*)(ws + off), __ATOMIC_RELAXED, __HIP_MEMORY_SCOPE_AGENT);
    if (__all((unsigned)(f >> 32) == tag)) break;
    __builtin_amdgcn_s_sleep(1);
  }
  asm volatile("" ::: "memory");
  return (unsigned)f;
}

#define STAMP(i) do {} while (0)
#define PART42 1
#define SIGF 7
#define HF0 3
#define NOW2 0
#define KSW 5
#define HPOLL 11
#define HCHK 14
#define HPS 4
#define TAUX 16
#define HAUX 16
#define ROT1 0
#define ROT2 0
#define ABL 0
#define YPOLL 15
#define YLAG 6
#define OUTAUX 16
#define HFIRST 6
#define HWIN 12

__global__ __launch_bounds__(512, 2) void fused_h3_mlp(const u16* __restrict__ gx, const u16* __restrict__ gk, const u16* __restrict__ gv,
                                                      const u16* __restrict__ gshk, const u16* __restrict__ gssk, const u16* __restrict__ gsD,
                                                      const u16* __restrict__ gw1, const u16* __restrict__ gb1, const u16* __restrict__ gw2,
                                                      const u16* __restrict__ gb2, u16* __restrict__ gout, char* __restrict__ ws) {
  STAMP(0);
  const int g = blockIdx.x, yy = blockIdx.y, zz = blockIdx.z;
  const int w = __builtin_amdgcn_readfirstlane(threadIdx.x >> 6);
  const int lane = threadIdx.x & 63, r = lane & 31, hh = lane >> 5;
  const i32x4 sWS = mksrd(ws, WS_END);
  asm volatile("" ::"s"(gx), "s"(gk), "s"(gv), "s"(gshk), "s"(gssk), "s"(gsD), "s"(gw1), "s"(gb1), "s"(gw2), "s"(gb2), "s"(gout), "s"(ws));
  u64 tag;
  if (yy == 7) {
    const int a = g + 8 * zz;
    const i32x4 sX = mksrd(gx, S_ * D_ * 2), sW1 = mksrd(gw1, D_ * F_ * 2);
    u32x4 xr[8], wv[7];
    const int row0 = 256 * g + 64 * zz + 8 * w;
    const unsigned xo = lane < 56 && !(ABL & 4) ? lane * 16 : 0x7fffffffu;
#pragma unroll
    for (int j = 0; j < 8; ++j)
      asm volatile("buffer_load_dwordx4 %0, %1, %2, %3 offen" : "=v"(xr[j]) : "v"(xo), "s"(sX), "s"((row0 + j) * 896) : "memory");
    const int ci = (g >> 1) + 4 * zz, cb = 14 * (g & 1) + ci;
    const unsigned wo = ci < 14 && !(ABL & 4) ? (lane >> 3) * 3584 + (lane & 7) * 16 + cb * 128 : 0x7fffffffu;
#pragma unroll
    for (int j = 0; j < 7; ++j)
      asm volatile("buffer_load_dwordx4 %0, %1, %2, %3 offen" : "=v"(wv[j]) : "v"(wo), "s"(sW1), "s"((56 * w + 8 * j) * 3584) : "memory");
    asm volatile("s_load_dwordx2 %0, %1, 0x0\n\ts_waitcnt lgkmcnt(0)" : "=s"(tag) : "s"(ws) : "memory");
    tag += 1ull;
    {
      float v[16];
#pragma unroll
      for (int j = 0; j < 8; ++j) {
        asm volatile("s_waitcnt vmcnt(%1)" : "+v"(xr[j]) : "i"(14 - j) : "memory");
        float s = 0.f, q = 0.f;
#pragma unroll
        for (int e = 0; e < 4; ++e) { s = dot2(xr[j][e], ONES, s); q = dot2(xr[j][e], xr[j][e], q); }
        v[j] = s; v[8 + j] = q;
        asm volatile("" : "+v"(v[j]), "+v"(v[8 + j]));
      }
#pragma unroll
      for (int j = 0; j < 8; ++j) { auto t = __builtin_amdgcn_permlane32_swap(__float_as_uint(v[j]), __float_as_uint(v[8 + j]), false, false); v[j] = __uint_as_float(t[0]) + __uint_as_float(t[1]); }
#pragma unroll
      for (int j = 0; j < 4; ++j) { auto t = __builtin_amdgcn_permlane16_swap(__float_as_uint(v[j]), __float_as_uint(v[4 + j]), false, false); v[j] = __uint_as_float(t[0]) + __uint_as_float(t[1]); }
#pragma unroll
      for (int j = 0; j < 4; ++j) { v[j] += ROR(v[j], 8); v[j] += ROR(v[j], 4); v[j] += ROR(v[j], 2); v[j] += ROR(v[j], 1); }
      const float mine = (lane & 2) ? ((lane & 1) ? v[3] : v[2]) : ((lane & 1) ? v[1] : v[0]);
      const auto t = __builtin_amdgcn_permlane32_swap(__float_as_uint(mine), __float_as_uint(mine), false, false);
      const float sq = __uint_as_float(t[1]);
      const float mean = mine * (1.f / D_);
      const float var = fmaxf(sq * (1.f / D_) - mean * mean, 0.f);
      const float den = __builtin_amdgcn_sqrtf(rbf(rbf(var) + 1e-5f));
      if ((lane & 44) == 0)
        __hip_atomic_store((GAS u64*)(ws + WS_STG + (row0 + ((lane >> 4) & 1) * 4 + (lane & 3)) * 8), ((u64)(unsigned)tag << 32) | pk(mean, den), __ATOMIC_RELAXED, __HIP_MEMORY_SCOPE_AGENT);
    }
    {
      unsigned onelo = 0x3f80u; OPAQUE(onelo);
      float c[8];
#pragma unroll
      for (int e = 0; e < 8; ++e) c[e] = 0.f;
#pragma unroll
      for (int j = 0; j < 7; ++j) {
        asm volatile("s_waitcnt vmcnt(%1)" : "+v"(wv[j]) : "i"(7 - j) : "memory");
#pragma unroll
        for (int e = 0; e < 4; ++e) { c[2 * e] = dot2(wv[j][e], onelo, c[2 * e]); c[2 * e + 1] = dot2(wv[j][e], ONE_HI, c[2 * e + 1]); }
        PIN8(c);
      }
#pragma unroll
      for (int e = 0; e < 4; ++e) { auto t = __builtin_amdgcn_permlane32_swap(__float_as_uint(c[e]), __float_as_uint(c[4 + e]), false, false); c[e] = __uint_as_float(t[0]) + __uint_as_float(t[1]); }
#pragma unroll
      for (int e = 0; e < 2; ++e) { auto t = __builtin_amdgcn_permlane16_swap(__float_as_uint(c[e]), __float_as_uint(c[2 + e]), false, false); c[e] = __uint_as_float(t[0]) + __uint_as_float(t[1]); }
      c[0] += ROR(c[0], 8); c[1] += ROR(c[1], 8);
      *(LAS f32x2*)(w * 256 + (lane & 7) * 32 + (lane >> 4) * 8) = f32x2{c[0], c[1]};
      BAR();
      if (w == 0 && ci < 14) {
        float t = 0.f;
#pragma unroll
        for (int j = 0; j < 8; ++j) t += *(const LAS float*)(j * 256 + lane * 4);
        __hip_atomic_store((GAS u64*)(ws + WS_CSG + (64 * cb + lane) * 8), ((u64)(unsigned)tag << 32) | __float_as_uint(t), __ATOMIC_RELAXED, __HIP_MEMORY_SCOPE_AGENT);
      }
    }
    if (a == 31) {
#pragma unroll 1
      for (int j = 0; j < 4; ++j) poll_gran(ws, WS_STG + ((w * 4 + j) * 64 + lane) * 8, (unsigned)tag);
#pragma unroll 1
      for (unsigned spins = 0; spins < SPIN_CAP; ++spins) {
        bool ok = true;
#pragma unroll
        for (int j = 0; j < 4; ++j) {
          const unsigned i = (w * 4 + j) * 64 + lane;
          ok &= __hip_atomic_load((GAS u64*)(ws + WS_HF + (i < 1792 ? i : 0) * FSTR), __ATOMIC_RELAXED, __HIP_MEMORY_SCOPE_AGENT) == tag;
        }
        if (__all(ok)) break;
        __builtin_amdgcn_s_sleep(8);
      }
      BAR();
      if (w == 0) flag_store(ws, WS_EPOCH, tag, lane);
    }
    return;
  }
  const int mt128 = 4 * (g >> 1) + zz, ft = 7 * (g & 1) + yy, hsel = g & 1;
  const int mt64 = 2 * mt128 + hsel, nt = yy;
  const int fg = w & 3, tg = w >> 2;
  const int cg = yy, tt = g + 8 * zz;
  const int ch0 = 64 * cg + 2 * tt;
  const unsigned wb = w * 2048, wb1 = w * 1024;
  {
    const unsigned vr8 = (lane >> 3) * 896 + (lane & 7) * 16, so = (64 * tt + 8 * w) * 896 + cg * 128;
    const i32x4 sK = mksrd(gk, S_ * D_ * 2);
    dma<L_SK + 1024, 0>(wb1, vr8, sK, so);
    dma<L_SV, 0>(wb1, vr8, mksrd(gv, S_ * D_ * 2), so);
    if (w == 0) dma<L_SK, 0>(0, tt ? vr8 : 0x7fffffffu, sK, tt ? so - 8 * 896 : 0);
    if (w == 1) dma<L_TP, 0>(0, lane * 16, mksrd(gshk, D_ * 8), cg * 512);
    dma<L_SW, 0>(wb1, vr8, mksrd(gw2, tt < 28 ? D_ * F_ * 2 : 0), so);
  }

  const i32x4 sX = mksrd(gx, S_ * D_ * 2), sW1 = mksrd(gw1, D_ * F_ * 2);
  unsigned vx0, vx1, vw0, vw1;
  {
    const int row = 16 * w + (lane >> 3);
    vx0 = (128 * mt128 + row) * 896 + (((lane ^ (lane >> 4)) & 7) << 4);
    vx1 = (vx0 + 8 * 896) ^ 64;
    const int q = lane >> 4;
    vw0 = (8 * w + q) * 3584 + ft * 256 + (((lane & 15) ^ ((q << 2) | (2 * (w & 1)))) << 4);
    vw1 = (vw0 + 4 * 3584) ^ 16;
  }
#define SLX(k) ((k) == 3 ? L_E + 16384 : L_X + ((k) & 3) * 16384)
#define SLW(k) ((k) == 3 ? L_E : L_W + ((k) & 3) * 16384)
  const int rot1 = ROT1 ? (yy + 2 * zz >= 7 ? yy + 2 * zz - 7 : yy + 2 * zz) : 0;
  auto kc = [&](int k) { const int q = k + rot1; return (unsigned)(q >= 7 ? q - 7 : q); };
#define DMA1(k, j) do { \
    if ((j) == 0) dma<SLX(k), 0>(wb, vx0, sX, kc(k) * 128); \
    if ((j) == 1) dma<SLW(k), 0>(wb, vw0, sW1, kc(k) * (64 * 3584)); \
    if ((j) == 2) dma<SLX(k) + 1024, 0>(wb, vx1, sX, kc(k) * 128); \
    if ((j) == 3) dma<SLW(k) + 1024, 0>(wb, vw1, sW1, kc(k) * (64 * 3584)); } while (0)
  __builtin_amdgcn_s_barrier();
  DMA1(0, 0); DMA1(0, 1); DMA1(0, 2); DMA1(0, 3);
  const auto rWS = __builtin_amdgcn_make_buffer_rsrc(ws, 0, WS_END, 0x00020000);
  unsigned dpair;
  asm volatile("s_load_dwordx2 %0, %2, 0x0\n\ts_load_dword %1, %3, 0x0" : "=&s"(tag), "=&s"(dpair) : "s"(ws), "s"(gsD + ch0) : "memory");

  unsigned aA0, aA1, aB[4];
  const int blk = (lane >> 4) & 1, q4 = (lane & 15) >> 2, p4 = lane & 3;
  {
    const int ch = 4 * fg + 2 * blk + (p4 >> 1);
    aA0 = L_W + 256 * (8 * hh + q4) + 16 * (ch ^ ((q4 << 2) | (2 * hh))) + 8 * (p4 & 1);
    aA1 = (aA0 + 1024) ^ 16;
    const unsigned b0 = L_X + (64 * tg + r) * 128 + ((hh ^ ((r >> 1) & 7)) << 4);
#pragma unroll
    for (int j = 0; j < 4; ++j) { aB[j] = b0 ^ (j << 5); OPAQUE(aB[j]); }
    OPAQUE(aA0); OPAQUE(aA1);
  }
  f32x16 acc0, acc1;
#pragma unroll
  for (int i = 0; i < 16; ++i) { acc0[i] = 0.f; acc1[i] = 0.f; }
  u32x4 fA[2], fB0[2], fB1[2];
  auto ldf = [&](int n) {
    const int j = n & 3, k = n >> 2;
    unsigned pa0 = aA0, pa1 = aA1, pb = aB[j];
    if (k == 3) { pa0 += L_E - L_W; pa1 += L_E - L_W; pb += L_E + 16384 - L_X; OPAQUE(pa0); OPAQUE(pa1); OPAQUE(pb); }
    const unsigned so = k == 3 ? 0 : (k & 3) * 16384;
    u32x2 a0 = ldstr(pa0 + so + j * 4096), a1 = ldstr(pa1 + so + j * 4096);
    fA[n & 1] = u32x4{a0[0], a0[1], a1[0], a1[1]};
    fB0[n & 1] = lds128(pb + so); fB1[n & 1] = lds128(pb + so + 4096);
  };
#define STEP(n) do { \
    SB(); acc0 = mfma(fA[(n) & 1], fB0[(n) & 1], acc0); asm volatile("" : "+v"(acc0)); SB(); \
    if ((n) + 1 < 28) ldf((n) + 1); \
    SB(); acc1 = mfma(fA[(n) & 1], fB1[(n) & 1], acc1); asm volatile("" : "+v"(acc1)); SB(); \
  } while (0)
#define PIECE(m) DMA1(((m) >> 2) + 2, (m) & 3)
  unsigned kf_so = WS_KVF + ((cg * 32 + tt) * 8 + w) * FSTR, hf_so = WS_HF + ((2 * mt128 + tg) * 56 + 4 * ft + fg) * FSTR, yf_so = WS_YF + ((cg * 8 + w) * 32 + tt) * FSTR;
  i32x4 sH = mksrd(gssk, S_ * D_ * 2);
  unsigned h_so = ch0 * 4096 + wb1, w2t_so = tt < 28 ? WS_W2T + (cg * 112 + 4 * tt) * 2048 + w * 1024 : WS_END;
  asm volatile("" : "+s"(kf_so), "+s"(hf_so), "+s"(yf_so), "+s"(sH), "+s"(h_so), "+s"(w2t_so));
  unsigned kvf_off = WS_KVF + (cg * 256 + (8 * fg + (lane & 7)) * 8 + (tt >> 2)) * FSTR;
  unsigned w2f_off = WS_KVF + (cg * 256 + 28 * w + (lane < 28 ? lane : 0)) * FSTR;
  unsigned gs_off = WS_STG + (128 * mt128 + 64 * tg + lane) * 8, gc_off = WS_CSG + (128 * ft + 32 * fg + r) * 8, b1_off = (128 * ft + 32 * fg + r) * 2;
  unsigned ks_off = (64 * cg + 8 * w + (lane >> 3)) * 4096 + (lane & 7) * 16, ko_rd = L_KO + (8 * w + (lane >> 3)) * 128 + (((lane ^ (lane >> 3)) & 7) << 4);
  unsigned l16 = lane * 16;
  asm volatile("" : "+v"(kvf_off), "+v"(w2f_off), "+v"(gs_off), "+v"(gc_off), "+v"(b1_off), "+v"(ks_off), "+v"(ko_rd), "+v"(l16));
  STAMP(1);
  asm volatile("s_waitcnt vmcnt(4) lgkmcnt(0)\n\ts_barrier" : "+s"(tag), "+s"(dpair) :: "memory");
  tag += 1ull;
  u32x2 tagv = {(unsigned)tag, (unsigned)(tag >> 32)};
  OPAQUE(tagv);
  STAMP(2);
#define TRYF(SENT, SO, Y, FORCE) do { if (!(SENT)) { const unsigned ib = __builtin_amdgcn_s_getreg(7 | (31 << 11)); \
      if ((FORCE) || ((ib & 15u) | ((ib >> 18) & 48u)) <= (Y)) { VMWAIT(Y); flag_put(sWS, SO, tagv); SENT = true; } } } while (0)
  bool ksent = false;
  {
    const unsigned ta = (8 * w + q4) * 128 + (lane >> 4) * 32 + p4 * 8;
    u32x2 k0 = ldstr(L_SK + 512 + ta), k1 = ldstr(L_SK + 1024 + ta), k2 = ldstr(L_SK + 1536 + ta);
    u32x2 v0 = ldstr(L_SV + ta), v1 = ldstr(L_SV + 512 + ta);
    u32x2 shk = lds64(L_TP + lane * 8);
    const unsigned wa = L_SW + (16 * (w >> 1) + 4 * hh + q4) * 128 + (w & 1) * 64 + blk * 32 + p4 * 8;
    const u32x2 wa0 = ldstr(wa), wa1 = ldstr(wa + 1024);
    DMA1(1, 0); DMA1(1, 1);
    BAR();
    DMA1(1, 2); DMA1(1, 3);
    const float kr[11] = {bfhi(k0[0]), bflo(k0[1]), bfhi(k0[1]), bflo(k1[0]), bfhi(k1[0]), bflo(k1[1]), bfhi(k1[1]), bflo(k2[0]), bfhi(k2[0]), bflo(k2[1]), bfhi(k2[1])};
    const float tw0 = bflo(shk[0]), tw1 = bfhi(shk[0]), tw2 = bflo(shk[1]), tw3 = bfhi(shk[1]);
    float a[8]; unsigned pp[8];
#pragma unroll
    for (int j = 0; j < 8; ++j) pp[j] = pk(fmul1(kr[j], tw3), fmul1(kr[j + 1], tw2));
    dot2x8(a, pp, ONES, ONES, ONES, ONES);
    PIECE(0);
#pragma unroll
    for (int j = 0; j < 8; ++j) pp[j] = pk(a[j], fmul1(kr[j + 2], tw1));
    dot2x8(a, pp, ONES, ONES, ONES, ONES);
    PIECE(1);
#pragma unroll
    for (int j = 0; j < 8; ++j) pp[j] = pk(a[j], fmul1(kr[j + 3], tw0));
    dot2x8(a, pp, ONES, ONES, ONES, ONES);
#pragma unroll
    for (int j = 0; j < 8; j += 2) { pp[j] = pk(a[j], 0.f); pp[j + 1] = pk(0.f, a[j + 1]); }
    dot2x8(a, pp, v0[0], v0[1], v1[0], v1[1]);
    sts128(L_KO + lane * 128 + ((w ^ (lane & 7)) << 4), u32x4{pk(a[7], a[6]), pk(a[5], a[4]), pk(a[3], a[2]), pk(a[1], a[0])});
    sts128(L_U + (fg >> 1) * U_SZ + ((fg & 1) * 2 + hh) * U_PL + r * 16, u32x4{0u, 0u, 0u, 0u});
    SB();
    STAMP(20);
    VMWAIT(6); BAR();
    STAMP(3);
    const u32x4 o = lds128(ko_rd);
    ldf(0);
    PIECE(2); PIECE(3);
    st128_sc1(u32x4{wa0[0], wa0[1], wa1[0], wa1[1]}, rWS, l16, w2t_so);
    st128_sc1(o, rWS, ks_off, WS_KVT + tt * 128);
  }
  STEP(0); PIECE(4); STEP(1); PIECE(5); STEP(2); PIECE(6);
  VMWAIT(9); BAR();
  STEP(3); PIECE(7); STEP(4); PIECE(8); STEP(5); PIECE(9); STEP(6); PIECE(10);
  VMWAIT(9); TRYF(ksent, kf_so, 7, false); BAR();
  STAMP(4);
  STEP(7); PIECE(11); STEP(8); PIECE(12); TRYF(ksent, kf_so, 9, false); STEP(9); PIECE(13); STEP(10); PIECE(14);
  VMWAIT(7); TRYF(ksent, kf_so, 11, true); BAR();
  STAMP(5);
  u32x2 pw, gs, gc; unsigned b1v = 0; u32x4 uld;
  STEP(11); PIECE(15); STEP(12); PIECE(16); STEP(13); PIECE(17); STEP(14); PIECE(18);
  VMWAIT(7);
  BAR();
  STAMP(6);
  u32x4 wf[28];
  const unsigned w2so = WS_W2T + (nt * 112 + 14 * w) * 2048;
  auto ldw2 = [&](int n0, int n1) {
#pragma unroll
    for (int n = n0; n < n1; ++n) { wf[n] = __builtin_amdgcn_raw_buffer_load_b128(rWS, l16 + (n & 3) * 1024, w2so + (n >> 2) * 4096, 16); SB(); }
  };
  STEP(15); PIECE(19);
  STEP(16); dma<L_SH, 0>(wb1, l16, sH, h_so);
  STEP(17); STEP(18);
  VMWAIT(5); BAR();
  STAMP(7);
  STEP(19);
  const unsigned pf_off = lane < 32 ? w2f_off : kvf_off;
  asm volatile("buffer_load_dwordx2 %0, %1, %2, 0 offen sc1" : "=v"(pw) : "v"(pf_off), "s"(sWS) : "memory");
  asm volatile("global_load_short_d16_hi %0, %1, %2" : "+v"(b1v) : "v"(b1_off), "s"(gb1) : "memory");
  asm volatile("buffer_load_dwordx2 %0, %1, %2, 0 offen sc1" : "=v"(gs) : "v"(gs_off), "s"(sWS) : "memory");
  asm volatile("buffer_load_dwordx2 %0, %1, %2, 0 offen sc1" : "=v"(gc) : "v"(gc_off), "s"(sWS) : "memory");
  STEP(20); STEP(21); STEP(22);
  VMWAIT(4);
  BAR();
  STAMP(8);
  STEP(23);
  unsigned ed[4][3];
  {
    unsigned ra = L_SH + w * 1024 + lane * 4;
    OPAQUE(ra);
#pragma unroll
    for (int j = 0; j < 4; ++j)
#pragma unroll
      for (int e = 0; e < 3; ++e) ed[j][e] = *(const LAS unsigned*)(ra + j * 256 + e * 4);
  }
  asm volatile("s_waitcnt vmcnt(0)" : "+v"(pw), "+v"(b1v), "+v"(gs), "+v"(gc) :: "memory");
  if (!__all((((u64)pw[1] << 32) | pw[0]) == tag)) poll_flags(ws, pf_off, tag);
  asm volatile("buffer_load_dwordx4 %0, %1, %2, %3 offen sc1" : "=v"(uld) : "v"(l16), "s"(sWS), "s"(WS_KVT + (ch0 + tg) * 4096 + fg * 1024) : "memory");
  STEP(24); ldw2(0, 2);
  STEP(25); ldw2(2, 4);
  STEP(26); ldw2(4, 6);
  STEP(27);
  if (!__all(gs[1] == (unsigned)tag && gc[1] == (unsigned)tag)) { gs[0] = poll_gran(ws, gs_off, (unsigned)tag); gc[0] = poll_gran(ws, gc_off, (unsigned)tag); }
  STAMP(10);
  {
    if (fg == 0) *(LAS unsigned*)(L_ST + (64 * tg + lane) * 4) = gs[0];
    const unsigned nm = gs[0] ^ 0x8000u;
    const unsigned bm = __builtin_amdgcn_perm(nm, nm, 0x01000100u);
    const float c = __uint_as_float(gc[0]), chi = rbf(c), c1 = c - chi, cmid = rbf(c1);
    const u32x4 A = {pk(chi, cmid), pk(c1 - cmid, __uint_as_float(b1v)), 0u, 0u};
    acc0 = mfma(A, u32x4{hh ? 0u : bm, hh ? 0u : nm, 0u, 0u}, acc0);
    acc1 = mfma(A, u32x4{hh ? bm : 0u, hh ? nm : 0u, 0u, 0u}, acc1);
    const unsigned so = WS_HT + ((4 * mt128 + 2 * tg) * 112 + 8 * ft + 2 * fg) * 1024;
    unsigned wa = L_E + 256 + tg * E_SZ + fg * 4096 + lane * 16;
    OPAQUE(wa);
#pragma unroll
    for (int q = 0; q < 4; ++q) {
      sts128(wa + q * 1024, u32x4{ed[q][0], ed[q][1], __builtin_amdgcn_alignbit(ed[q][1], ed[q][0], 16), __builtin_amdgcn_alignbit(ed[q][2], ed[q][1], 16)});
      u32x4 hv;
#pragma unroll
      for (int e = 0; e < 4; ++e) {
        float a0 = (q < 2 ? acc0 : acc1)[8 * (q & 1) + 2 * e], a1 = (q < 2 ? acc0 : acc1)[8 * (q & 1) + 2 * e + 1];
        a0 *= __builtin_fabsf(a0); OPAQUE(a0);
        a1 *= __builtin_fabsf(a1);
        const s16x2 m = __builtin_elementwise_max(__builtin_bit_cast(s16x2, pk(a0, a1)), s16x2{0, 0});
        hv[e] = __builtin_bit_cast(unsigned, m);
      }
      st128_sc1(hv, rWS, (ABL & 8) ? 0x7fffffffu : l16 + (q & 1) * 1024, so + (q >> 1) * 112 * 1024);
    }
  }
  STAMP(11);
  asm volatile("s_waitcnt vmcnt(10)" : "+v"(uld) :: "memory");
  {
    const int n = 64 * fg + lane;
    sts128(L_U + tg * U_SZ + (n & 3) * U_PL + ((n >> 2) + 32) * 16, uld);
    if (fg == 0) {
      const unsigned h0 = __builtin_amdgcn_readfirstlane(ed[0][0]), h1 = __builtin_amdgcn_readfirstlane(__builtin_amdgcn_alignbit(ed[0][1], ed[0][0], 16));
      const int l = lane & 15;
      sts128(L_E + tg * E_SZ + l * 16, u32x4{0u, l == 15 ? h0 : 0u, l == 15 ? h0 << 16 : 0u, l == 15 ? h1 : l == 14 ? h0 << 16 : 0u});
    }
  }
  ldw2(6, 8);
  BAR();
  STAMP(21);
  const int tok = threadIdx.x >> 4, quad = threadIdx.x & 15;
  const unsigned hf_off = WS_HF + (mt64 * 56 + 7 * w + (lane < 7 ? lane : 6)) * FSTR;
  const unsigned xo = ((64 * mt64 + tok) * D_ + 64 * nt + 4 * quad) * 2;
  u32x2 hf, xr0, xr1, b2r;
  constexpr int WIN = HWIN, NH0 = (16 - HCHK) * HPS;
  u32x4 hb[WIN];
  const unsigned hso = WS_HT + (2 * mt64 * 112 + 14 * w) * 1024;
  auto ldh = [&](int n) {
    const int mt = n / 14, ks = n % 14;
    hb[n % WIN] = __builtin_amdgcn_raw_buffer_load_b128(rWS, l16, hso + mt * 112 * 1024 + ks * 1024, 16);
    SB();
  };
  {
    f32x16 c0, c1;
#pragma unroll
    for (int i = 0; i < 16; ++i) { c0[i] = 0.f; c1[i] = 0.f; }
    unsigned ea0 = L_E + tg * E_SZ + 8 * (32 + r - 8 * hh - 7 + 256 * fg - 16), ea1 = ea0 + 32, ea2 = ea0 + 8192, ea3 = ea0 + 8224;
    unsigned ua = L_U + tg * U_SZ + hh * U_PL + 16 * (r + 32 - 8 * fg - 7);
    OPAQUE(ea0); OPAQUE(ea1); OPAQUE(ea2); OPAQUE(ea3); OPAQUE(ua);
    u32x4 A0[2], A1[2], B0[2], B1[2];
    auto ldca = [&](int st) {
      const int dd = st >> 1, s = st & 1;
      const unsigned eo = 8 * (32 * dd + 16 * (1 - s)), uo = 2 * s * U_PL + 16 * (7 - dd);
      u32x2 x0 = lds64(ea0 + eo), x1 = lds64(ea1 + eo);
      B0[st & 1] = lds128(ua + uo);
      A0[st & 1] = u32x4{x0[0], x0[1], x1[0], x1[1]};
    };
    auto ldcb = [&](int st) {
      const int dd = st >> 1, s = st & 1;
      const unsigned eo = 8 * (32 * dd + 16 * (1 - s)), uo = 2 * s * U_PL + 16 * (7 - dd);
      B1[st & 1] = lds128(ua + uo + 512);
      u32x2 y0 = lds64(ea2 + eo), y1 = lds64(ea3 + eo);
      A1[st & 1] = u32x4{y0[0], y0[1], y1[0], y1[1]};
    };
    ldca(0); ldcb(0);
    SB();
    bool hsent = false;
#pragma unroll
    for (int st = 0; st < 16; ++st) {
      c0 = mfma(A0[st & 1], B0[st & 1], c0); asm volatile("" : "+v"(c0)); SB();
      if (st + 1 < 16) ldca(st + 1);
      SB();
      c1 = mfma(A0[st & 1], B1[st & 1], c1); asm volatile("" : "+v"(c1)); SB();
      if (st + 1 < 16) ldcb(st + 1);
      SB();
      c1 = mfma(A1[st & 1], B0[st & 1], c1); asm volatile("" : "+v"(c1)); SB();
      if (st >= HF0 && st <= SIGF && (st & 1) && !hsent) {
        const int younger = 2 + 2 * st;
        const unsigned ib = __builtin_amdgcn_s_getreg(7 | (31 << 11));
        if (st == SIGF || ((ib & 15u) | ((ib >> 18) & 48u)) <= (unsigned)younger) {
          VMWAIT(younger);
          flag_put(sWS, hf_so, tagv);
          hsent = true;
        }
      }
      if (st < 10) ldw2(8 + 2 * st, 10 + 2 * st);
      if (st == 10) {
        asm volatile("buffer_load_dwordx2 %0, %1, %2, 0 offen" : "=v"(xr0) : "v"(xo), "s"(sX) : "memory");
        asm volatile("buffer_load_dwordx2 %0, %1, %2, %3 offen" : "=v"(xr1) : "v"(xo), "s"(sX), "s"(32 * 896) : "memory");
        asm volatile("global_load_dwordx2 %0, %1, %2" : "=v"(b2r) : "v"((64 * nt + 4 * quad) * 2), "s"(gb2) : "memory");
      }
      if (st == HPOLL) asm volatile("buffer_load_dwordx2 %0, %1, %2, 0 offen sc1" : "=v"(hf) : "v"(hf_off), "s"(sWS) : "memory");
      if (st == HCHK) {
        STAMP(9);
        asm volatile("s_waitcnt vmcnt(0)" : "+v"(hf), "+v"(xr0), "+v"(xr1), "+v"(b2r) :: "memory");
        if (!__all((((u64)hf[1] << 32) | hf[0]) == tag)) poll_flags(ws, hf_off, tag);
        STAMP(22);
      }
      if (st >= HCHK) {
#pragma unroll
        for (int n = (st - HCHK) * HPS; n < (st - HCHK + 1) * HPS; ++n) ldh(n);
      }
    }
    STAMP(12);
    const unsigned pb = L_CP + w * P_SZ + lane * 16;
#pragma unroll
    for (int g4 = 0; g4 < 4; ++g4) {
      *(LAS f32x4*)(pb + g4 * 1088) = f32x4{c0[4 * g4], c0[4 * g4 + 1], c0[4 * g4 + 2], c0[4 * g4 + 3]};
      *(LAS f32x4*)(pb + 4352 + g4 * 1088) = f32x4{c1[4 * g4], c1[4 * g4 + 1], c1[4 * g4 + 2], c1[4 * g4 + 3]};
    }
  }
  BAR();
  STAMP(13);
  {
    const int n = 32 * w + r, a = n >> 2, g4 = n & 3;
    const unsigned po = L_CP + hh * 4 * P_SZ + (a >> 5) * 4352 + g4 * 1088 + (a & 31) * 16;
    f32x4 lo = *(const LAS f32x4*)po, hi = *(const LAS f32x4*)(po + 512);
#pragma unroll
    for (int q = 1; q < 4; ++q) {
      lo += *(const LAS f32x4*)(po + q * P_SZ);
      hi += *(const LAS f32x4*)(po + q * P_SZ + 512);
    }
    const u32x4 kvc = lds128(L_U + hh * U_SZ + g4 * U_PL + (a + 32) * 16);
    const unsigned dlo = hh ? dpair >> 16 : dpair & 0xffffu, dhi = dlo << 16;
    float pd[8], yv[8];
    { const unsigned kk[8] = {kvc[3], kvc[2], kvc[1], kvc[0], kvc[3], kvc[2], kvc[1], kvc[0]}; dot2x8(pd, kk, dhi, dhi, dlo, dlo); }
    { const unsigned pp[8] = {pk(lo[0], pd[0]), pk(lo[1], pd[4]), pk(lo[2], pd[1]), pk(lo[3], pd[5]), pk(hi[0], pd[2]), pk(hi[1], pd[6]), pk(hi[2], pd[3]), pk(hi[3], pd[7])};
      dot2x8(yv, pp, ONES, ONES, ONES, ONES); }
    const u32x4 yo = {pk(yv[0], yv[1]), pk(yv[2], yv[3]), pk(yv[4], yv[5]), pk(yv[6], yv[7])};
    st128_sc1(yo, rWS, (ch0 + hh) * 4096 + n * 16, WS_YT);
  }
  STAMP(14);
#pragma unroll
  for (int n = NH0; n < WIN; ++n) ldh(n);
  auto gate = [&](unsigned ra, float r2, unsigned (&gt)[2]) {
    f32x4 sm = *(const LAS f32x4*)ra;
#pragma unroll
    for (int q = 1; q < 8; ++q) sm += *(const LAS f32x4*)(ra + q * G_SZ);
    const unsigned oa = pk(fmaf(sm[0], r2, bflo(b2r[0])), fmaf(sm[1], r2, bfhi(b2r[0]))), ob = pk(fmaf(sm[2], r2, bflo(b2r[1])), fmaf(sm[3], r2, bfhi(b2r[1])));
    const float o[4] = {bflo(oa), bfhi(oa), bflo(ob), bfhi(ob)};
    float sv[4];
#pragma unroll
    for (int e = 0; e < 4; ++e) sv[e] = o[e] * __builtin_amdgcn_rcpf(1.f + __builtin_amdgcn_exp2f(o[e] * -1.4426950408889634f));
    gt[0] = pk(sv[0], sv[1]); gt[1] = pk(sv[2], sv[3]);
  };
  auto xnorm = [&](u32x2 xr, unsigned st, unsigned (&xn)[2], float& r2, float* xf) {
    const float m = bflo(st), rd = __builtin_amdgcn_rcpf(bfhi(st));
    xf[0] = bflo(xr[0]); xf[1] = bfhi(xr[0]); xf[2] = bflo(xr[1]); xf[3] = bfhi(xr[1]);
    const unsigned da = pk(xf[0] - m, xf[1] - m), db = pk(xf[2] - m, xf[3] - m);
    xn[0] = pk(bflo(da) * rd, bfhi(da) * rd); xn[1] = pk(bflo(db) * rd, bfhi(db) * rd);
    r2 = rd * rd;
  };
  unsigned g0[2], g1[2], xn0[2], xn1[2];
  float r20, r21, xf[8];
  unsigned rp0 = L_G0 + quad * G_H + tok * 16, rp1 = rp0 - (L_G0 - L_G1), yra = L_Y + 4 * quad * 132 + tok * 2;
  const unsigned yf_off = WS_YF + ((nt * 8 + (mt64 >> 2)) * 32 + 4 * w + (lane & 3)) * FSTR;
  u32x2 yf; u32x4 yb;
  const unsigned yvo = (lane >> 3) * 4096 + (lane & 7) * 16, yso = WS_YT + (64 * nt + 8 * w) * 4096 + mt64 * 128;
  STAMP(15);
  {
    f32x16 ca[2], cb[2];
    f32x4 gp[4], gq[4]; float gs4[4], ge[4];
    const float nl2e = -1.4426950408889634f;
#pragma unroll
    for (int i = 0; i < 16; ++i) { ca[0][i] = 0.f; cb[0][i] = 0.f; ca[1][i] = 0.f; cb[1][i] = 0.f; }
    unsigned pb0 = L_G0 + w * G_SZ + hh * G_H + r * 16, pb1 = pb0 - (L_G0 - L_G1);
    OPAQUE(pb0); OPAQUE(pb1);
    auto wrp = [&](unsigned pb, const f32x16& c, int g4, int t) { *(LAS f32x4*)(pb + t * G_T + g4 * G_G) = f32x4{c[4 * g4], c[4 * g4 + 1], c[4 * g4 + 2], c[4 * g4 + 3]}; };
#pragma unroll
    for (int n = 0; n < 28; ++n) {
      const int mt = n / 14, ks = n % 14;
      if (n == NH0) { VMWAIT(WIN - 1); flag_put(sWS, yf_so, tagv); }
      ca[mt] = mfma(wf[2 * ks], hb[n % WIN], ca[mt]); asm volatile("" : "+v"(ca[mt])); SB();
      if (n >= 1 && n - 1 + WIN < 28) ldh(n - 1 + WIN);
      if (n >= 14 && n < 18) { wrp(pb0, ca[0], n - 14, 0); SB(); }
      cb[mt] = mfma(wf[2 * ks + 1], hb[n % WIN], cb[mt]); asm volatile("" : "+v"(cb[mt])); SB();
      if (n == 1) {
        const unsigned st0 = *(const LAS unsigned*)(L_ST + (64 * hsel + tok) * 4), st1 = *(const LAS unsigned*)(L_ST + (64 * hsel + 32 + tok) * 4);
        xnorm(xr0, st0, xn0, r20, xf); xnorm(xr1, st1, xn1, r21, xf + 4);
        asm volatile("" : "+v"(xn0[0]), "+v"(xn0[1]), "+v"(xn1[0]), "+v"(xn1[1]), "+v"(r20), "+v"(r21), "+v"(rp0), "+v"(rp1), "+v"(yra)); PIN8(xf); SB();
      }
      if (n >= 14 && n < 18) { wrp(pb0, cb[0], n - 14, 1); SB(); }
      if (n == 20) { BAR(); STAMP(16); }
      if (n == 20 || n == 21) {
#pragma unroll
        for (int q = 0; q < 4; ++q) gp[q] = *(const LAS f32x4*)(rp0 + (4 * (n - 20) + q) * G_SZ);
        SB();
      }
      if (n == 21 || n == 22) {
#pragma unroll
        for (int e = 0; e < 4; ++e) { const float t = fadd1(fadd1(gq[0][e], gq[1][e]), fadd1(gq[2][e], gq[3][e])); gs4[e] = n == 21 ? t : fadd1(gs4[e], t); }
        asm volatile("" : "+v"(gs4[0]), "+v"(gs4[1]), "+v"(gs4[2]), "+v"(gs4[3])); SB();
      }
      if (n == 20 || n == 21) {
#pragma unroll
        for (int q = 0; q < 4; ++q) gq[q] = gp[q];
      }
      if (n == 23) {
        const unsigned oa = pk(ffma1(gs4[0], r20, bflo(b2r[0])), ffma1(gs4[1], r20, bfhi(b2r[0]))), ob = pk(ffma1(gs4[2], r20, bflo(b2r[1])), ffma1(gs4[3], r20, bfhi(b2r[1])));
        gs4[0] = bflo(oa); gs4[1] = bfhi(oa); gs4[2] = bflo(ob); gs4[3] = bfhi(ob);
        asm volatile("" : "+v"(gs4[0]), "+v"(gs4[1]), "+v"(gs4[2]), "+v"(gs4[3])); SB();
      }
      if (n == 24) {
#pragma unroll
        for (int e = 0; e < 4; ++e) ge[e] = __builtin_amdgcn_exp2f(fmul1(gs4[e], nl2e));
        asm volatile("" : "+v"(ge[0]), "+v"(ge[1]), "+v"(ge[2]), "+v"(ge[3])); SB();
      }
      if (n == 25) {
#pragma unroll
        for (int e = 0; e < 4; ++e) ge[e] = __builtin_amdgcn_rcpf(1.f + ge[e]);
        asm volatile("" : "+v"(ge[0]), "+v"(ge[1]), "+v"(ge[2]), "+v"(ge[3])); SB();
      }
      if (n == 26) {
        g0[0] = pk(fmul1(gs4[0], ge[0]), fmul1(gs4[1], ge[1])); g0[1] = pk(fmul1(gs4[2], ge[2]), fmul1(gs4[3], ge[3]));
        asm volatile("" : "+v"(g0[0]), "+v"(g0[1])); SB();
      }
      if (n == YPOLL) asm volatile("buffer_load_dwordx2 %0, %1, %2, 0 offen sc1" : "=v"(yf) : "v"(yf_off), "s"(sWS) : "memory");
      if (n == YPOLL + YLAG) {
        asm volatile("s_waitcnt vmcnt(0)" : "+v"(yf) :: "memory");
        asm volatile("buffer_load_dwordx4 %0, %1, %2, %3 offen sc1" : "=v"(yb) : "v"(yvo), "s"(sWS), "s"(yso) : "memory");
      }
    }
#pragma unroll
    for (int g4 = 0; g4 < 4; ++g4) { wrp(pb1, ca[1], g4, 0); wrp(pb1, cb[1], g4, 1); }
  }
  STAMP(17);
  asm volatile("s_waitcnt vmcnt(0)" : "+v"(yb) :: "memory");
  if (!__all((((u64)yf[1] << 32) | yf[0]) == tag)) {
    poll_flags(ws, yf_off, tag);
    asm volatile("buffer_load_dwordx4 %0, %1, %2, %3 offen sc1\n\ts_waitcnt vmcnt(0)" : "=&v"(yb) : "v"(yvo), "s"(sWS), "s"(yso) : "memory");
  }
  {
    const unsigned ya = L_Y + (8 * w + (lane >> 3)) * 132 + (lane & 7) * 16;
#pragma unroll
    for (int e = 0; e < 4; ++e) *(LAS unsigned*)(ya + 4 * e) = yb[e];
  }
  BAR();
  STAMP(18);
  {
    unsigned yv[8];
#pragma unroll
    for (int e = 0; e < 8; ++e) yv[e] = *(const LAS u16*)(yra + (e & 3) * 132 + (e >> 2) * 64);
    gate(rp1, r21, g1);
    float t[8]; unsigned pp[8];
#pragma unroll
    for (int e = 1; e < 8; e += 2) yv[e] <<= 16;
    dot2x8(t, yv, xn0[0], xn0[1], xn1[0], xn1[1]);
#pragma unroll
    for (int e = 0; e < 8; e += 2) { pp[e] = pk(t[e], 0.f); pp[e + 1] = pk(0.f, t[e + 1]); }
    dot2x8(t, pp, g0[0], g0[1], g1[0], g1[1]);
#pragma unroll
    for (int e = 0; e < 8; ++e) pp[e] = pk(t[e], xf[e]);
    dot2x8(t, pp, ONES, ONES, ONES, ONES);
    const auto rO = __builtin_amdgcn_make_buffer_rsrc(gout, 0, S_ * D_ * 2, 0x00020000);
    __builtin_amdgcn_raw_buffer_store_b64(u32x2{pk(t[0], t[1]), pk(t[2], t[3])}, rO, xo, 0, OUTAUX);
    __builtin_amdgcn_raw_buffer_store_b64(u32x2{pk(t[4], t[5]), pk(t[6], t[7])}, rO, xo, 32 * 896, OUTAUX);
  }
}

extern "C" void kernel_launch(void* const* d_in, const int* in_sizes, int n_in, void* d_out, int out_size, void* d_ws, size_t ws_size,
                              hipStream_t stream) {
  (void)in_sizes; (void)n_in; (void)out_size; (void)ws_size;
  (void)hipFuncSetAttribute((const void*)fused_h3_mlp, hipFuncAttributeMaxDynamicSharedMemorySize, LDS_BYTES);
  fused_h3_mlp<<<dim3(8, 8, 4), 512, LDS_BYTES, stream>>>((const u16*)d_in[0], (const u16*)d_in[1], (const u16*)d_in[2], (const u16*)d_in[3],
                                                         (const u16*)d_in[4], (const u16*)d_in[5], (const u16*)d_in[7], (const u16*)d_in[8],
                                                         (const u16*)d_in[9], (const u16*)d_in[10], (u16*)d_out, (char*)d_ws);
}
```

```cpp
#include <hip/hip_runtime.h>
#include <hip/hip_bf16.h>
#include <stdint.h>
#include <type_traits>

typedef unsigned short u16;
typedef unsigned long long u64;
typedef __bf16 bf16x8 __attribute__((ext_vector_type(8)));
typedef __bf16 bf16x2 __attribute__((ext_vector_type(2)));
typedef short s16x2 __attribute__((ext_vector_type(2)));
typedef short s16x4 __attribute__((ext_vector_type(4)));
typedef float f32x2 __attribute__((ext_vector_type(2)));
typedef float f32x4 __attribute__((ext_vector_type(4)));
typedef float f32x16 __attribute__((ext_vector_type(16)));
typedef unsigned u32x2 __attribute__((ext_vector_type(2)));
typedef unsigned u32x4 __attribute__((ext_vector_type(4)));
typedef int i32x4 __attribute__((ext_vector_type(4)));
#define GAS __attribute__((address_space(1)))
#define LAS __attribute__((address_space(3)))
#define DI __device__ __forceinline__
#pragma clang diagnostic ignored "-Wint-to-pointer-cast"

constexpr int S_ = 2048, D_ = 448, F_ = 1792;
constexpr unsigned FSTR = 256;
constexpr unsigned WS_EPOCH = 0;
constexpr unsigned WS_KVF = 4096;
constexpr unsigned WS_HF = WS_KVF + 1792 * FSTR;
constexpr unsigned WS_YF = WS_HF + 1792 * FSTR;
constexpr unsigned WS_STG = WS_YF + 1792 * FSTR;
constexpr unsigned WS_CSG = WS_STG + 2048 * 8;
constexpr unsigned WS_KVT = 2u << 20;
constexpr unsigned WS_YT = 4u << 20;
constexpr unsigned WS_HT = 6u << 20;
constexpr unsigned WS_W2T = WS_HT + 64u * 112u * 1024u;
constexpr unsigned WS_END = WS_W2T + 7u * 112u * 2048u;
static_assert(WS_CSG + 1792 * 8 <= WS_KVT, "ws");
constexpr unsigned L_X = 49152;
constexpr unsigned L_W = 0;
constexpr unsigned L_E = 98304;
constexpr unsigned E_SZ = 16640;
constexpr unsigned U_PL = 1568, U_SZ = 4 * U_PL;
constexpr unsigned L_SH = L_E + 2 * E_SZ;
constexpr unsigned L_SS = L_SH + 8192 + 64;
constexpr unsigned L_KV = L_SS + 1024;
constexpr unsigned L_Y = L_KV;
constexpr unsigned L_ST = L_KV + 9216;
constexpr unsigned L_U = L_ST + 1024;
constexpr unsigned P_SZ = 8704;
constexpr unsigned L_CP = 0;
constexpr unsigned L_G0 = 69632;
constexpr unsigned G_SZ = 8448;
constexpr unsigned L_G1 = 0;
constexpr unsigned L_SK = L_U;
constexpr unsigned L_TP = L_U + 9216;
constexpr unsigned L_SV = L_KV;
constexpr unsigned L_KO = L_KV;
constexpr unsigned L_SW = L_SH;
constexpr unsigned G_H = 528, G_G = 1056, G_T = 4224;
constexpr unsigned LDS_BYTES = L_U + 2 * U_SZ;
static_assert(L_G0 + 8 * G_SZ <= L_KV && L_U % 16 == 0, "lds");
static_assert(LDS_BYTES <= 163840, "lds");

constexpr unsigned SPIN_CAP = 1u << 18;
constexpr unsigned ONES = 0x3f803f80u, ONE_HI = 0x3f800000u;

DI float bflo(unsigned v) { return __uint_as_float(v << 16); }
DI float bfhi(unsigned v) { return __uint_as_float(v & 0xffff0000u); }
DI unsigned pk(float lo, float hi) { f32x2 f = {lo, hi}; return __builtin_bit_cast(unsigned, __builtin_convertvector(f, bf16x2)); }
DI float fmul1(float a, float b) { float d; asm("v_mul_f32 %0, %1, %2" : "=v"(d) : "v"(a), "v"(b)); return d; }
DI float fadd1(float a, float b) { float d; asm("v_add_f32 %0, %1, %2" : "=v"(d) : "v"(a), "v"(b)); return d; }
DI float ffma1(float a, float b, float c) { float d; asm("v_fma_f32 %0, %1, %2, %3" : "=v"(d) : "v"(a), "v"(b), "v"(c)); return d; }
DI float rbf(float a) { return bfhi(pk(a, a)); }
DI float dot2(unsigned a, unsigned b, float c) { return __builtin_amdgcn_fdot2_f32_bf16(__builtin_bit_cast(bf16x2, a), __builtin_bit_cast(bf16x2, b), c, false); }
DI void dot2x8(float (&d)[8], const unsigned (&a)[8], unsigned b0, unsigned b1, unsigned b2, unsigned b3) {
  asm volatile("v_dot2_f32_bf16 %0, %8, %16, 0\n\tv_dot2_f32_bf16 %1, %9, %16, 0\n\tv_dot2_f32_bf16 %2, %10, %17, 0\n\tv_dot2_f32_bf16 %3, %11, %17, 0\n\t"
               "v_dot2_f32_bf16 %4, %12, %18, 0\n\tv_dot2_f32_bf16 %5, %13, %18, 0\n\tv_dot2_f32_bf16 %6, %14, %19, 0\n\tv_dot2_f32_bf16 %7, %15, %19, 0\n\ts_nop 2"
               : "=&v"(d[0]), "=&v"(d[1]), "=&v"(d[2]), "=&v"(d[3]), "=&v"(d[4]), "=&v"(d[5]), "=&v"(d[6]), "=&v"(d[7])
               : "v"(a[0]), "v"(a[1]), "v"(a[2]), "v"(a[3]), "v"(a[4]), "v"(a[5]), "v"(a[6]), "v"(a[7]), "v"(b0), "v"(b1), "v"(b2), "v"(b3));
}
DI f32x16 mfma(u32x4 a, u32x4 b, f32x16 c) { return __builtin_amdgcn_mfma_f32_32x32x16_bf16(__builtin_bit_cast(bf16x8, a), __builtin_bit_cast(bf16x8, b), c, 0, 0, 0); }
DI u32x4 lds128(unsigned a) { return *(const LAS u32x4*)a; }
DI u32x2 lds64(unsigned a) { return *(const LAS u32x2*)a; }
DI u32x2 ldstr(unsigned a) { return __builtin_bit_cast(u32x2, __builtin_amdgcn_ds_read_tr16_b64_v4i16((LAS s16x4*)a)); }
DI void sts128(unsigned a, u32x4 v) { *(LAS u32x4*)a = v; }
DI i32x4 mksrd(const void* p, unsigned bytes) {
  u64 a = (u64)p;
  i32x4 r = {(int)(unsigned)a, (int)((unsigned)(a >> 32) & 0xffffu), (int)bytes, 0x00020000};
  return r;
}
#define VMWAIT(n) asm volatile("s_waitcnt vmcnt(%0)" ::"i"(n) : "memory")
#define BAR() asm volatile("s_waitcnt lgkmcnt(0)\n\ts_barrier" ::: "memory")
#define OPAQUE(x) asm volatile("" : "+v"(x))
#define SB() __builtin_amdgcn_sched_barrier(0)
#define ROR(x, n) __uint_as_float(__builtin_amdgcn_update_dpp(0u, __float_as_uint(x), 0x120 + (n), 0xf, 0xf, false))
#define PIN8(a) asm volatile("" : "+v"(a[0]), "+v"(a[1]), "+v"(a[2]), "+v"(a[3]), "+v"(a[4]), "+v"(a[5]), "+v"(a[6]), "+v"(a[7]))
template <int LOFF, int GOFF> DI void dma(unsigned wb, unsigned voff, i32x4 srd, unsigned soff) {
  asm volatile("s_add_i32 m0, %0, %4\n\ts_nop 0\n\tbuffer_load_dwordx4 %1, %2, %3 offen offset:%5 lds"
               ::"s"(wb), "v"(voff), "s"(srd), "s"(soff), "i"(LOFF - GOFF), "i"(GOFF) : "scc");
}
template <typename R> DI void st128_sc1(u32x4 v, R rsrc, unsigned voff, unsigned soff) {
  __builtin_amdgcn_raw_buffer_store_b128(v, rsrc, voff, soff, 16);
  asm volatile("s_nop 1" ::"v"(v) : "memory");
}
DI void flag_store(char* ws, unsigned off, u64 tag, int lane) {
  if (lane == 0) __hip_atomic_store((GAS u64*)(ws + off), tag, __ATOMIC_RELAXED, __HIP_MEMORY_SCOPE_AGENT);
}
DI void flag_put(i32x4 srd, unsigned soff, u32x2 tagv) { asm volatile("buffer_store_dwordx2 %0, off, %1, %2 sc1" ::"v"(tagv), "s"(srd), "s"(soff) : "memory"); }
DI void poll_flags(char* ws, unsigned off, u64 tag) {
#pragma unroll 1
  for (unsigned spins = 0; spins < SPIN_CAP; ++spins) {
    u64 f = __hip_atomic_load((GAS u64*)(ws + off), __ATOMIC_RELAXED, __HIP_MEMORY_SCOPE_AGENT);
    if (__all(f == tag)) break;
    __builtin_amdgcn_s_sleep(1);
  }
  asm volatile("" ::: "memory");
}

DI unsigned poll_gran(char* ws, unsigned off, unsigned tag) {
  u64 f = 0;
#pragma unroll 1
  for (unsigned spins = 0; spins < SPIN_CAP; ++spins) {
    f = __hip_atomic_load((GAS u64*)(ws + off), __ATOMIC_RELAXED, __HIP_MEMORY_SCOPE_AGENT);
    if (__all((unsigned)(f >> 32) == tag)) break;
    __builtin_amdgcn_s_sleep(1);
  }
  asm volatile("" ::: "memory");
  return (unsigned)f;
}

constexpr int HF0 = 3, SIGF = 7;
constexpr int HPOLL = 11, HCHK = 14, HPS = 4;
constexpr int YPOLL = 15, YLAG = 6;
constexpr int HWIN = 12;

__global__ __launch_bounds__(512, 2) void fused_h3_mlp(const u16* __restrict__ gx, const u16* __restrict__ gk, const u16* __restrict__ gv,
                                                      const u16* __restrict__ gshk, const u16* __restrict__ gssk, const u16* __restrict__ gsD,
                                                      const u16* __restrict__ gw1, const u16* __restrict__ gb1, const u16* __restrict__ gw2,
                                                      const u16* __restrict__ gb2, u16* __restrict__ gout, char* __restrict__ ws) {
  const int g = blockIdx.x, yy = blockIdx.y, zz = blockIdx.z;
  const int w = __builtin_amdgcn_readfirstlane(threadIdx.x >> 6);
  const int lane = threadIdx.x & 63, r = lane & 31, hh = lane >> 5;
  const i32x4 sWS = mksrd(ws, WS_END);
  asm volatile("" ::"s"(gx), "s"(gk), "s"(gv), "s"(gshk), "s"(gssk), "s"(gsD), "s"(gw1), "s"(gb1), "s"(gw2), "s"(gb2), "s"(gout), "s"(ws));
  u64 tag;
  if (yy == 7) {
    const int a = g + 8 * zz;
    const i32x4 sX = mksrd(gx, S_ * D_ * 2), sW1 = mksrd(gw1, D_ * F_ * 2);
    u32x4 xr[8], wv[7];
    const int row0 = 256 * g + 64 * zz + 8 * w;
    const unsigned xo = lane < 56 ? lane * 16 : 0x7fffffffu;
#pragma unroll
    for (int j = 0; j < 8; ++j)
      asm volatile("buffer_load_dwordx4 %0, %1, %2, %3 offen" : "=v"(xr[j]) : "v"(xo), "s"(sX), "s"((row0 + j) * 896) : "memory");
    const int ci = (g >> 1) + 4 * zz, cb = 14 * (g & 1) + ci;
    const unsigned wo = ci < 14 ? (lane >> 3) * 3584 + (lane & 7) * 16 + cb * 128 : 0x7fffffffu;
#pragma unroll
    for (int j = 0; j < 7; ++j)
      asm volatile("buffer_load_dwordx4 %0, %1, %2, %3 offen" : "=v"(wv[j]) : "v"(wo), "s"(sW1), "s"((56 * w + 8 * j) * 3584) : "memory");
    asm volatile("s_load_dwordx2 %0, %1, 0x0\n\ts_waitcnt lgkmcnt(0)" : "=s"(tag) : "s"(ws) : "memory");
    tag += 1ull;
    {
      float v[16];
#pragma unroll
      for (int j = 0; j < 8; ++j) {
        asm volatile("s_waitcnt vmcnt(%1)" : "+v"(xr[j]) : "i"(14 - j) : "memory");
        float s = 0.f, q = 0.f;
#pragma unroll
        for (int e = 0; e < 4; ++e) { s = dot2(xr[j][e], ONES, s); q = dot2(xr[j][e], xr[j][e], q); }
        v[j] = s; v[8 + j] = q;
        asm volatile("" : "+v"(v[j]), "+v"(v[8 + j]));
      }
#pragma unroll
      for (int j = 0; j < 8; ++j) { auto t = __builtin_amdgcn_permlane32_swap(__float_as_uint(v[j]), __float_as_uint(v[8 + j]), false, false); v[j] = __uint_as_float(t[0]) + __uint_as_float(t[1]); }
#pragma unroll
      for (int j = 0; j < 4; ++j) { auto t = __builtin_amdgcn_permlane16_swap(__float_as_uint(v[j]), __float_as_uint(v[4 + j]), false, false); v[j] = __uint_as_float(t[0]) + __uint_as_float(t[1]); }
#pragma unroll
      for (int j = 0; j < 4; ++j) { v[j] += ROR(v[j], 8); v[j] += ROR(v[j], 4); v[j] += ROR(v[j], 2); v[j] += ROR(v[j], 1); }
      const float mine = (lane & 2) ? ((lane & 1) ? v[3] : v[2]) : ((lane & 1) ? v[1] : v[0]);
      const auto t = __builtin_amdgcn_permlane32_swap(__float_as_uint(mine), __float_as_uint(mine), false, false);
      const float sq = __uint_as_float(t[1]);
      const float mean = mine * (1.f / D_);
      const float var = fmaxf(sq * (1.f / D_) - mean * mean, 0.f);
      const float den = __builtin_amdgcn_sqrtf(rbf(rbf(var) + 1e-5f));
      if ((lane & 44) == 0)
        __hip_atomic_store((GAS u64*)(ws + WS_STG + (row0 + ((lane >> 4) & 1) * 4 + (lane & 3)) * 8), ((u64)(unsigned)tag << 32) | pk(mean, den), __ATOMIC_RELAXED, __HIP_MEMORY_SCOPE_AGENT);
    }
    {
      unsigned onelo = 0x3f80u; OPAQUE(onelo);
      float c[8];
#pragma unroll
      for (int e = 0; e < 8; ++e) c[e] = 0.f;
#pragma unroll
      for (int j = 0; j < 7; ++j) {
        asm volatile("s_waitcnt vmcnt(%1)" : "+v"(wv[j]) : "i"(7 - j) : "memory");
#pragma unroll
        for (int e = 0; e < 4; ++e) { c[2 * e] = dot2(wv[j][e], onelo, c[2 * e]); c[2 * e + 1] = dot2(wv[j][e], ONE_HI, c[2 * e + 1]); }
        PIN8(c);
      }
#pragma unroll
      for (int e = 0; e < 4; ++e) { auto t = __builtin_amdgcn_permlane32_swap(__float_as_uint(c[e]), __float_as_uint(c[4 + e]), false, false); c[e] = __uint_as_float(t[0]) + __uint_as_float(t[1]); }
#pragma unroll
      for (int e = 0; e < 2; ++e) { auto t = __builtin_amdgcn_permlane16_swap(__float_as_uint(c[e]), __float_as_uint(c[2 + e]), false, false); c[e] = __uint_as_float(t[0]) + __uint_as_float(t[1]); }
      c[0] += ROR(c[0], 8); c[1] += ROR(c[1], 8);
      *(LAS f32x2*)(w * 256 + (lane & 7) * 32 + (lane >> 4) * 8) = f32x2{c[0], c[1]};
      BAR();
      if (w == 0 && ci < 14) {
        float t = 0.f;
#pragma unroll
        for (int j = 0; j < 8; ++j) t += *(const LAS float*)(j * 256 + lane * 4);
        __hip_atomic_store((GAS u64*)(ws + WS_CSG + (64 * cb + lane) * 8), ((u64)(unsigned)tag << 32) | __float_as_uint(t), __ATOMIC_RELAXED, __HIP_MEMORY_SCOPE_AGENT);
      }
    }
    if (a == 31) {
#pragma unroll 1
      for (int j = 0; j < 4; ++j) poll_gran(ws, WS_STG + ((w * 4 + j) * 64 + lane) * 8, (unsigned)tag);
#pragma unroll 1
      for (unsigned spins = 0; spins < SPIN_CAP; ++spins) {
        bool ok = true;
#pragma unroll
        for (int j = 0; j < 4; ++j) {
          const unsigned i = (w * 4 + j) * 64 + lane;
          ok &= __hip_atomic_load((GAS u64*)(ws + WS_HF + (i < 1792 ? i : 0) * FSTR), __ATOMIC_RELAXED, __HIP_MEMORY_SCOPE_AGENT) == tag;
        }
        if (__all(ok)) break;
        __builtin_amdgcn_s_sleep(8);
      }
      BAR();
      if (w == 0) flag_store(ws, WS_EPOCH, tag, lane);
    }
    return;
  }
  const int mt128 = 4 * (g >> 1) + zz, ft = 7 * (g & 1) + yy, hsel = g & 1;
  const int mt64 = 2 * mt128 + hsel, nt = yy;
  const int fg = w & 3, tg = w >> 2;
  const int cg = yy, tt = g + 8 * zz;
  const int ch0 = 64 * cg + 2 * tt;
  const unsigned wb = w * 2048, wb1 = w * 1024;
  {
    const unsigned vr8 = (lane >> 3) * 896 + (lane & 7) * 16, so = (64 * tt + 8 * w) * 896 + cg * 128;
    const i32x4 sK = mksrd(gk, S_ * D_ * 2);
    dma<L_SK + 1024, 0>(wb1, vr8, sK, so);
    dma<L_SV, 0>(wb1, vr8, mksrd(gv, S_ * D_ * 2), so);
    if (w == 0) dma<L_SK, 0>(0, tt ? vr8 : 0x7fffffffu, sK, tt ? so - 8 * 896 : 0);
    if (w == 1) dma<L_TP, 0>(0, lane * 16, mksrd(gshk, D_ * 8), cg * 512);
    dma<L_SW, 0>(wb1, vr8, mksrd(gw2, tt < 28 ? D_ * F_ * 2 : 0), so);
  }

  const i32x4 sX = mksrd(gx, S_ * D_ * 2), sW1 = mksrd(gw1, D_ * F_ * 2);
  unsigned vx0, vx1, vw0, vw1;
  {
    const int row = 16 * w + (lane >> 3);
    vx0 = (128 * mt128 + row) * 896 + (((lane ^ (lane >> 4)) & 7) << 4);
    vx1 = (vx0 + 8 * 896) ^ 64;
    const int q = lane >> 4;
    vw0 = (8 * w + q) * 3584 + ft * 256 + (((lane & 15) ^ ((q << 2) | (2 * (w & 1)))) << 4);
    vw1 = (vw0 + 4 * 3584) ^ 16;
  }
#define SLX(k) ((k) == 3 ? L_E + 16384 : L_X + ((k) & 3) * 16384)
#define SLW(k) ((k) == 3 ? L_E : L_W + ((k) & 3) * 16384)
#define DMA1(k, j) do { \
    if ((j) == 0) dma<SLX(k), (k) * 128>(wb, vx0, sX, 0); \
    if ((j) == 1) dma<SLW(k), 0>(wb, vw0, sW1, (k) * 64 * 3584); \
    if ((j) == 2) dma<SLX(k) + 1024, (k) * 128>(wb, vx1, sX, 0); \
    if ((j) == 3) dma<SLW(k) + 1024, 0>(wb, vw1, sW1, (k) * 64 * 3584); } while (0)
  __builtin_amdgcn_s_barrier();
  DMA1(0, 0); DMA1(0, 1); DMA1(0, 2); DMA1(0, 3);
  const auto rWS = __builtin_amdgcn_make_buffer_rsrc(ws, 0, WS_END, 0x00020000);
  unsigned dpair;
  asm volatile("s_load_dwordx2 %0, %2, 0x0\n\ts_load_dword %1, %3, 0x0" : "=&s"(tag), "=&s"(dpair) : "s"(ws), "s"(gsD + ch0) : "memory");

  unsigned aA0, aA1, aB[4];
  const int blk = (lane >> 4) & 1, q4 = (lane & 15) >> 2, p4 = lane & 3;
  {
    const int ch = 4 * fg + 2 * blk + (p4 >> 1);
    aA0 = L_W + 256 * (8 * hh + q4) + 16 * (ch ^ ((q4 << 2) | (2 * hh))) + 8 * (p4 & 1);
    aA1 = (aA0 + 1024) ^ 16;
    const unsigned b0 = L_X + (64 * tg + r) * 128 + ((hh ^ ((r >> 1) & 7)) << 4);
#pragma unroll
    for (int j = 0; j < 4; ++j) { aB[j] = b0 ^ (j << 5); OPAQUE(aB[j]); }
    OPAQUE(aA0); OPAQUE(aA1);
  }
  f32x16 acc0, acc1;
#pragma unroll
  for (int i = 0; i < 16; ++i) { acc0[i] = 0.f; acc1[i] = 0.f; }
  u32x4 fA[2], fB0[2], fB1[2];
  auto ldf = [&](int n) {
    const int j = n & 3, k = n >> 2;
    unsigned pa0 = aA0, pa1 = aA1, pb = aB[j];
    if (k == 3) { pa0 += L_E - L_W; pa1 += L_E - L_W; pb += L_E + 16384 - L_X; OPAQUE(pa0); OPAQUE(pa1); OPAQUE(pb); }
    const unsigned so = k == 3 ? 0 : (k & 3) * 16384;
    u32x2 a0 = ldstr(pa0 + so + j * 4096), a1 = ldstr(pa1 + so + j * 4096);
    fA[n & 1] = u32x4{a0[0], a0[1], a1[0], a1[1]};
    fB0[n & 1] = lds128(pb + so); fB1[n & 1] = lds128(pb + so + 4096);
  };
#define STEP(n) do { \
    SB(); acc0 = mfma(fA[(n) & 1], fB0[(n) & 1], acc0); asm volatile("" : "+v"(acc0)); SB(); \
    if ((n) + 1 < 28) ldf((n) + 1); \
    SB(); acc1 = mfma(fA[(n) & 1], fB1[(n) & 1], acc1); asm volatile("" : "+v"(acc1)); SB(); \
  } while (0)
#define PIECE(m) DMA1(((m) >> 2) + 2, (m) & 3)
  unsigned kf_so = WS_KVF + ((cg * 32 + tt) * 8 + w) * FSTR, hf_so = WS_HF + ((2 * mt128 + tg) * 56 + 4 * ft + fg) * FSTR, yf_so = WS_YF + ((cg * 8 + w) * 32 + tt) * FSTR;
  i32x4 sH = mksrd(gssk, S_ * D_ * 2);
  unsigned h_so = ch0 * 4096 + wb1, w2t_so = tt < 28 ? WS_W2T + (cg * 112 + 4 * tt) * 2048 + w * 1024 : WS_END;
  asm volatile("" : "+s"(kf_so), "+s"(hf_so), "+s"(yf_so), "+s"(sH), "+s"(h_so), "+s"(w2t_so));
  unsigned kvf_off = WS_KVF + (cg * 256 + (8 * fg + (lane & 7)) * 8 + (tt >> 2)) * FSTR;
  unsigned w2f_off = WS_KVF + (cg * 256 + 28 * w + (lane < 28 ? lane : 0)) * FSTR;
  unsigned gs_off = WS_STG + (128 * mt128 + 64 * tg + lane) * 8, gc_off = WS_CSG + (128 * ft + 32 * fg + r) * 8, b1_off = (128 * ft + 32 * fg + r) * 2;
  unsigned ks_off = (64 * cg + 8 * w + (lane >> 3)) * 4096 + (lane & 7) * 16, ko_rd = L_KO + (8 * w + (lane >> 3)) * 128 + (((lane ^ (lane >> 3)) & 7) << 4);
  unsigned l16 = lane * 16;
  asm volatile("" : "+v"(kvf_off), "+v"(w2f_off), "+v"(gs_off), "+v"(gc_off), "+v"(b1_off), "+v"(ks_off), "+v"(ko_rd), "+v"(l16));
  asm volatile("s_waitcnt vmcnt(4) lgkmcnt(0)\n\ts_barrier" : "+s"(tag), "+s"(dpair) :: "memory");
  tag += 1ull;
  u32x2 tagv = {(unsigned)tag, (unsigned)(tag >> 32)};
  OPAQUE(tagv);
#define TRYF(SENT, SO, Y, FORCE) do { if (!(SENT)) { const unsigned ib = __builtin_amdgcn_s_getreg(7 | (31 << 11)); \
      if ((FORCE) || ((ib & 15u) | ((ib >> 18) & 48u)) <= (Y)) { VMWAIT(Y); flag_put(sWS, SO, tagv); SENT = true; } } } while (0)
  bool ksent = false;
  {
    const unsigned ta = (8 * w + q4) * 128 + (lane >> 4) * 32 + p4 * 8;
    u32x2 k0 = ldstr(L_SK + 512 + ta), k1 = ldstr(L_SK + 1024 + ta), k2 = ldstr(L_SK + 1536 + ta);
    u32x2 v0 = ldstr(L_SV + ta), v1 = ldstr(L_SV + 512 + ta);
    u32x2 shk = lds64(L_TP + lane * 8);
    const unsigned wa = L_SW + (16 * (w >> 1) + 4 * hh + q4) * 128 + (w & 1) * 64 + blk * 32 + p4 * 8;
    const u32x2 wa0 = ldstr(wa), wa1 = ldstr(wa + 1024);
    DMA1(1, 0); DMA1(1, 1);
    BAR();
    DMA1(1, 2); DMA1(1, 3);
    const float kr[11] = {bfhi(k0[0]), bflo(k0[1]), bfhi(k0[1]), bflo(k1[0]), bfhi(k1[0]), bflo(k1[1]), bfhi(k1[1]), bflo(k2[0]), bfhi(k2[0]), bflo(k2[1]), bfhi(k2[1])};
    const float tw0 = bflo(shk[0]), tw1 = bfhi(shk[0]), tw2 = bflo(shk[1]), tw3 = bfhi(shk[1]);
    float a[8]; unsigned pp[8];
#pragma unroll
    for (int j = 0; j < 8; ++j) pp[j] = pk(fmul1(kr[j], tw3), fmul1(kr[j + 1], tw2));
    dot2x8(a, pp, ONES, ONES, ONES, ONES);
    PIECE(0);
#pragma unroll
    for (int j = 0; j < 8; ++j) pp[j] = pk(a[j], fmul1(kr[j + 2], tw1));
    dot2x8(a, pp, ONES, ONES, ONES, ONES);
    PIECE(1);
#pragma unroll
    for (int j = 0; j < 8; ++j) pp[j] = pk(a[j], fmul1(kr[j + 3], tw0));
    dot2x8(a, pp, ONES, ONES, ONES, ONES);
#pragma unroll
    for (int j = 0; j < 8; j += 2) { pp[j] = pk(a[j], 0.f); pp[j + 1] = pk(0.f, a[j + 1]); }
    dot2x8(a, pp, v0[0], v0[1], v1[0], v1[1]);
    sts128(L_KO + lane * 128 + ((w ^ (lane & 7)) << 4), u32x4{pk(a[7], a[6]), pk(a[5], a[4]), pk(a[3], a[2]), pk(a[1], a[0])});
    sts128(L_U + (fg >> 1) * U_SZ + ((fg & 1) * 2 + hh) * U_PL + r * 16, u32x4{0u, 0u, 0u, 0u});
    SB();
    VMWAIT(6); BAR();
    const u32x4 o = lds128(ko_rd);
    ldf(0);
    PIECE(2); PIECE(3);
    st128_sc1(u32x4{wa0[0], wa0[1], wa1[0], wa1[1]}, rWS, l16, w2t_so);
    st128_sc1(o, rWS, ks_off, WS_KVT + tt * 128);
  }
  STEP(0); PIECE(4); STEP(1); PIECE(5); STEP(2); PIECE(6);
  VMWAIT(9); BAR();
  STEP(3); PIECE(7); STEP(4); PIECE(8); STEP(5); PIECE(9); STEP(6); PIECE(10);
  VMWAIT(9); TRYF(ksent, kf_so, 7, false); BAR();
  STEP(7); PIECE(11); STEP(8); PIECE(12); TRYF(ksent, kf_so, 9, false); STEP(9); PIECE(13); STEP(10); PIECE(14);
  VMWAIT(7); TRYF(ksent, kf_so, 11, true); BAR();
  u32x2 pw, gs, gc; unsigned b1v = 0; u32x4 uld;
  STEP(11); PIECE(15); STEP(12); PIECE(16); STEP(13); PIECE(17); STEP(14); PIECE(18);
  VMWAIT(7);
  BAR();
  u32x4 wf[28];
  const unsigned w2so = WS_W2T + (nt * 112 + 14 * w) * 2048;
  auto ldw2 = [&](int n0, int n1) {
#pragma unroll
    for (int n = n0; n < n1; ++n) { wf[n] = __builtin_amdgcn_raw_buffer_load_b128(rWS, l16 + (n & 3) * 1024, w2so + (n >> 2) * 4096, 16); SB(); }
  };
  STEP(15); PIECE(19);
  STEP(16); dma<L_SH, 0>(wb1, l16, sH, h_so);
  STEP(17); STEP(18);
  VMWAIT(5); BAR();
  STEP(19);
  const unsigned pf_off = lane < 32 ? w2f_off : kvf_off;
  asm volatile("buffer_load_dwordx2 %0, %1, %2, 0 offen sc1" : "=v"(pw) : "v"(pf_off), "s"(sWS) : "memory");
  asm volatile("global_load_short_d16_hi %0, %1, %2" : "+v"(b1v) : "v"(b1_off), "s"(gb1) : "memory");
  asm volatile("buffer_load_dwordx2 %0, %1, %2, 0 offen sc1" : "=v"(gs) : "v"(gs_off), "s"(sWS) : "memory");
  asm volatile("buffer_load_dwordx2 %0, %1, %2, 0 offen sc1" : "=v"(gc) : "v"(gc_off), "s"(sWS) : "memory");
  STEP(20); STEP(21); STEP(22);
  VMWAIT(4);
  BAR();
  STEP(23);
  unsigned ed[4][3];
  {
    unsigned ra = L_SH + w * 1024 + lane * 4;
    OPAQUE(ra);
#pragma unroll
    for (int j = 0; j < 4; ++j)
#pragma unroll
      for (int e = 0; e < 3; ++e) ed[j][e] = *(const LAS unsigned*)(ra + j * 256 + e * 4);
  }
  asm volatile("s_waitcnt vmcnt(0)" : "+v"(pw), "+v"(b1v), "+v"(gs), "+v"(gc) :: "memory");
  if (!__all((((u64)pw[1] << 32) | pw[0]) == tag)) poll_flags(ws, pf_off, tag);
  asm volatile("buffer_load_dwordx4 %0, %1, %2, %3 offen sc1" : "=v"(uld) : "v"(l16), "s"(sWS), "s"(WS_KVT + (ch0 + tg) * 4096 + fg * 1024) : "memory");
  STEP(24); ldw2(0, 2);
  STEP(25); ldw2(2, 4);
  STEP(26); ldw2(4, 6);
  STEP(27);
  if (!__all(gs[1] == (unsigned)tag && gc[1] == (unsigned)tag)) { gs[0] = poll_gran(ws, gs_off, (unsigned)tag); gc[0] = poll_gran(ws, gc_off, (unsigned)tag); }
  {
    if (fg == 0) *(LAS unsigned*)(L_ST + (64 * tg + lane) * 4) = gs[0];
    const unsigned nm = gs[0] ^ 0x8000u;
    const unsigned bm = __builtin_amdgcn_perm(nm, nm, 0x01000100u);
    const float c = __uint_as_float(gc[0]), chi = rbf(c), c1 = c - chi, cmid = rbf(c1);
    const u32x4 A = {pk(chi, cmid), pk(c1 - cmid, __uint_as_float(b1v)), 0u, 0u};
    acc0 = mfma(A, u32x4{hh ? 0u : bm, hh ? 0u : nm, 0u, 0u}, acc0);
    acc1 = mfma(A, u32x4{hh ? bm : 0u, hh ? nm : 0u, 0u, 0u}, acc1);
    const unsigned so = WS_HT + ((4 * mt128 + 2 * tg) * 112 + 8 * ft + 2 * fg) * 1024;
    unsigned wa = L_E + 256 + tg * E_SZ + fg * 4096 + lane * 16;
    OPAQUE(wa);
#pragma unroll
    for (int q = 0; q < 4; ++q) {
      sts128(wa + q * 1024, u32x4{ed[q][0], ed[q][1], __builtin_amdgcn_alignbit(ed[q][1], ed[q][0], 16), __builtin_amdgcn_alignbit(ed[q][2], ed[q][1], 16)});
      u32x4 hv;
#pragma unroll
      for (int e = 0; e < 4; ++e) {
        float a0 = (q < 2 ? acc0 : acc1)[8 * (q & 1) + 2 * e], a1 = (q < 2 ? acc0 : acc1)[8 * (q & 1) + 2 * e + 1];
        a0 *= __builtin_fabsf(a0); OPAQUE(a0);
        a1 *= __builtin_fabsf(a1);
        const s16x2 m = __builtin_elementwise_max(__builtin_bit_cast(s16x2, pk(a0, a1)), s16x2{0, 0});
        hv[e] = __builtin_bit_cast(unsigned, m);
      }
      st128_sc1(hv, rWS, l16 + (q & 1) * 1024, so + (q >> 1) * 112 * 1024);
    }
  }
  asm volatile("s_waitcnt vmcnt(10)" : "+v"(uld) :: "memory");
  {
    const int n = 64 * fg + lane;
    sts128(L_U + tg * U_SZ + (n & 3) * U_PL + ((n >> 2) + 32) * 16, uld);
    if (fg == 0) {
      const unsigned h0 = __builtin_amdgcn_readfirstlane(ed[0][0]), h1 = __builtin_amdgcn_readfirstlane(__builtin_amdgcn_alignbit(ed[0][1], ed[0][0], 16));
      const int l = lane & 15;
      sts128(L_E + tg * E_SZ + l * 16, u32x4{0u, l == 15 ? h0 : 0u, l == 15 ? h0 << 16 : 0u, l == 15 ? h1 : l == 14 ? h0 << 16 : 0u});
    }
  }
  ldw2(6, 8);
  BAR();
  const int tok = threadIdx.x >> 4, quad = threadIdx.x & 15;
  const unsigned hf_off = WS_HF + (mt64 * 56 + 7 * w + (lane < 7 ? lane : 6)) * FSTR;
  const unsigned xo = ((64 * mt64 + tok) * D_ + 64 * nt + 4 * quad) * 2;
  u32x2 hf, xr0, xr1, b2r;
  constexpr int WIN = HWIN, NH0 = (16 - HCHK) * HPS;
  u32x4 hb[WIN];
  const unsigned hso = WS_HT + (2 * mt64 * 112 + 14 * w) * 1024;
  auto ldh = [&](int n) {
    const int mt = n / 14, ks = n % 14;
    hb[n % WIN] = __builtin_amdgcn_raw_buffer_load_b128(rWS, l16, hso + mt * 112 * 1024 + ks * 1024, 16);
    SB();
  };
  {
    f32x16 c0, c1;
#pragma unroll
    for (int i = 0; i < 16; ++i) { c0[i] = 0.f; c1[i] = 0.f; }
    unsigned ea0 = L_E + tg * E_SZ + 8 * (32 + r - 8 * hh - 7 + 256 * fg - 16), ea1 = ea0 + 32, ea2 = ea0 + 8192, ea3 = ea0 + 8224;
    unsigned ua = L_U + tg * U_SZ + hh * U_PL + 16 * (r + 32 - 8 * fg - 7);
    OPAQUE(ea0); OPAQUE(ea1); OPAQUE(ea2); OPAQUE(ea3); OPAQUE(ua);
    u32x4 A0[2], A1[2], B0[2], B1[2];
    auto ldca = [&](int st) {
      const int dd = st >> 1, s = st & 1;
      const unsigned eo = 8 * (32 * dd + 16 * (1 - s)), uo = 2 * s * U_PL + 16 * (7 - dd);
      u32x2 x0 = lds64(ea0 + eo), x1 = lds64(ea1 + eo);
      B0[st & 1] = lds128(ua + uo);
      A0[st & 1] = u32x4{x0[0], x0[1], x1[0], x1[1]};
    };
    auto ldcb = [&](int st) {
      const int dd = st >> 1, s = st & 1;
      const unsigned eo = 8 * (32 * dd + 16 * (1 - s)), uo = 2 * s * U_PL + 16 * (7 - dd);
      B1[st & 1] = lds128(ua + uo + 512);
      u32x2 y0 = lds64(ea2 + eo), y1 = lds64(ea3 + eo);
      A1[st & 1] = u32x4{y0[0], y0[1], y1[0], y1[1]};
    };
    ldca(0); ldcb(0);
    SB();
    bool hsent = false;
#pragma unroll
    for (int st = 0; st < 16; ++st) {
      c0 = mfma(A0[st & 1], B0[st & 1], c0); asm volatile("" : "+v"(c0)); SB();
      if (st + 1 < 16) ldca(st + 1);
      SB();
      c1 = mfma(A0[st & 1], B1[st & 1], c1); asm volatile("" : "+v"(c1)); SB();
      if (st + 1 < 16) ldcb(st + 1);
      SB();
      c1 = mfma(A1[st & 1], B0[st & 1], c1); asm volatile("" : "+v"(c1)); SB();
      if (st >= HF0 && st <= SIGF && (st & 1) && !hsent) {
        const int younger = 2 + 2 * st;
        const unsigned ib = __builtin_amdgcn_s_getreg(7 | (31 << 11));
        if (st == SIGF || ((ib & 15u) | ((ib >> 18) & 48u)) <= (unsigned)younger) {
          VMWAIT(younger);
          flag_put(sWS, hf_so, tagv);
          hsent = true;
        }
      }
      if (st < 10) ldw2(8 + 2 * st, 10 + 2 * st);
      if (st == 10) {
        asm volatile("buffer_load_dwordx2 %0, %1, %2, 0 offen" : "=v"(xr0) : "v"(xo), "s"(sX) : "memory");
        asm volatile("buffer_load_dwordx2 %0, %1, %2, %3 offen" : "=v"(xr1) : "v"(xo), "s"(sX), "s"(32 * 896) : "memory");
        asm volatile("global_load_dwordx2 %0, %1, %2" : "=v"(b2r) : "v"((64 * nt + 4 * quad) * 2), "s"(gb2) : "memory");
      }
      if (st == HPOLL) asm volatile("buffer_load_dwordx2 %0, %1, %2, 0 offen sc1" : "=v"(hf) : "v"(hf_off), "s"(sWS) : "memory");
      if (st == HCHK) {
        asm volatile("s_waitcnt vmcnt(0)" : "+v"(hf), "+v"(xr0), "+v"(xr1), "+v"(b2r) :: "memory");
        if (!__all((((u64)hf[1] << 32) | hf[0]) == tag)) poll_flags(ws, hf_off, tag);
      }
      if (st >= HCHK) {
#pragma unroll
        for (int n = (st - HCHK) * HPS; n < (st - HCHK + 1) * HPS; ++n) ldh(n);
      }
    }
    const unsigned pb = L_CP + w * P_SZ + lane * 16;
#pragma unroll
    for (int g4 = 0; g4 < 4; ++g4) {
      *(LAS f32x4*)(pb + g4 * 1088) = f32x4{c0[4 * g4], c0[4 * g4 + 1], c0[4 * g4 + 2], c0[4 * g4 + 3]};
      *(LAS f32x4*)(pb + 4352 + g4 * 1088) = f32x4{c1[4 * g4], c1[4 * g4 + 1], c1[4 * g4 + 2], c1[4 * g4 + 3]};
    }
  }
  BAR();
  {
    const int n = 32 * w + r, a = n >> 2, g4 = n & 3;
    const unsigned po = L_CP + hh * 4 * P_SZ + (a >> 5) * 4352 + g4 * 1088 + (a & 31) * 16;
    f32x4 lo = *(const LAS f32x4*)po, hi = *(const LAS f32x4*)(po + 512);
#pragma unroll
    for (int q = 1; q < 4; ++q) {
      lo += *(const LAS f32x4*)(po + q * P_SZ);
      hi += *(const LAS f32x4*)(po + q * P_SZ + 512);
    }
    const u32x4 kvc = lds128(L_U + hh * U_SZ + g4 * U_PL + (a + 32) * 16);
    const unsigned dlo = hh ? dpair >> 16 : dpair & 0xffffu, dhi = dlo << 16;
    float pd[8], yv[8];
    { const unsigned kk[8] = {kvc[3], kvc[2], kvc[1], kvc[0], kvc[3], kvc[2], kvc[1], kvc[0]}; dot2x8(pd, kk, dhi, dhi, dlo, dlo); }
    { const unsigned pp[8] = {pk(lo[0], pd[0]), pk(lo[1], pd[4]), pk(lo[2], pd[1]), pk(lo[3], pd[5]), pk(hi[0], pd[2]), pk(hi[1], pd[6]), pk(hi[2], pd[3]), pk(hi[3], pd[7])};
      dot2x8(yv, pp, ONES, ONES, ONES, ONES); }
    const u32x4 yo = {pk(yv[0], yv[1]), pk(yv[2], yv[3]), pk(yv[4], yv[5]), pk(yv[6], yv[7])};
    st128_sc1(yo, rWS, (ch0 + hh) * 4096 + n * 16, WS_YT);
  }
#pragma unroll
  for (int n = NH0; n < WIN; ++n) ldh(n);
  auto gate = [&](unsigned ra, float r2, unsigned (&gt)[2]) {
    f32x4 sm = *(const LAS f32x4*)ra;
#pragma unroll
    for (int q = 1; q < 8; ++q) sm += *(const LAS f32x4*)(ra + q * G_SZ);
    const unsigned oa = pk(fmaf(sm[0], r2, bflo(b2r[0])), fmaf(sm[1], r2, bfhi(b2r[0]))), ob = pk(fmaf(sm[2], r2, bflo(b2r[1])), fmaf(sm[3], r2, bfhi(b2r[1])));
    const float o[4] = {bflo(oa), bfhi(oa), bflo(ob), bfhi(ob)};
    float sv[4];
#pragma unroll
    for (int e = 0; e < 4; ++e) sv[e] = o[e] * __builtin_amdgcn_rcpf(1.f + __builtin_amdgcn_exp2f(o[e] * -1.4426950408889634f));
    gt[0] = pk(sv[0], sv[1]); gt[1] = pk(sv[2], sv[3]);
  };
  auto xnorm = [&](u32x2 xr, unsigned st, unsigned (&xn)[2], float& r2, float* xf) {
    const float m = bflo(st), rd = __builtin_amdgcn_rcpf(bfhi(st));
    xf[0] = bflo(xr[0]); xf[1] = bfhi(xr[0]); xf[2] = bflo(xr[1]); xf[3] = bfhi(xr[1]);
    const unsigned da = pk(xf[0] - m, xf[1] - m), db = pk(xf[2] - m, xf[3] - m);
    xn[0] = pk(bflo(da) * rd, bfhi(da) * rd); xn[1] = pk(bflo(db) * rd, bfhi(db) * rd);
    r2 = rd * rd;
  };
  unsigned g0[2], g1[2], xn0[2], xn1[2];
  float r20, r21, xf[8];
  unsigned rp0 = L_G0 + quad * G_H + tok * 16, rp1 = rp0 - (L_G0 - L_G1), yra = L_Y + 4 * quad * 132 + tok * 2;
  const unsigned yf_off = WS_YF + ((nt * 8 + (mt64 >> 2)) * 32 + 4 * w + (lane & 3)) * FSTR;
  u32x2 yf; u32x4 yb;
  const unsigned yvo = (lane >> 3) * 4096 + (lane & 7) * 16, yso = WS_YT + (64 * nt + 8 * w) * 4096 + mt64 * 128;
  {
    f32x16 ca[2], cb[2];
    f32x4 gp[4], gq[4]; float gs4[4], ge[4];
    const float nl2e = -1.4426950408889634f;
#pragma unroll
    for (int i = 0; i < 16; ++i) { ca[0][i] = 0.f; cb[0][i] = 0.f; ca[1][i] = 0.f; cb[1][i] = 0.f; }
    unsigned pb0 = L_G0 + w * G_SZ + hh * G_H + r * 16, pb1 = pb0 - (L_G0 - L_G1);
    OPAQUE(pb0); OPAQUE(pb1);
    auto wrp = [&](unsigned pb, const f32x16& c, int g4, int t) { *(LAS f32x4*)(pb + t * G_T + g4 * G_G) = f32x4{c[4 * g4], c[4 * g4 + 1], c[4 * g4 + 2], c[4 * g4 + 3]}; };
#pragma unroll
    for (int n = 0; n < 28; ++n) {
      const int mt = n / 14, ks = n % 14;
      if (n == NH0) { VMWAIT(WIN - 1); flag_put(sWS, yf_so, tagv); }
      ca[mt] = mfma(wf[2 * ks], hb[n % WIN], ca[mt]); asm volatile("" : "+v"(ca[mt])); SB();
      if (n >= 1 && n - 1 + WIN < 28) ldh(n - 1 + WIN);
      if (n >= 14 && n < 18) { wrp(pb0, ca[0], n - 14, 0); SB(); }
      cb[mt] = mfma(wf[2 * ks + 1], hb[n % WIN], cb[mt]); asm volatile("" : "+v"(cb[mt])); SB();
      if (n == 1) {
        const unsigned st0 = *(const LAS unsigned*)(L_ST + (64 * hsel + tok) * 4), st1 = *(const LAS unsigned*)(L_ST + (64 * hsel + 32 + tok) * 4);
        xnorm(xr0, st0, xn0, r20, xf); xnorm(xr1, st1, xn1, r21, xf + 4);
        asm volatile("" : "+v"(xn0[0]), "+v"(xn0[1]), "+v"(xn1[0]), "+v"(xn1[1]), "+v"(r20), "+v"(r21), "+v"(rp0), "+v"(rp1), "+v"(yra)); PIN8(xf); SB();
      }
      if (n >= 14 && n < 18) { wrp(pb0, cb[0], n - 14, 1); SB(); }
      if (n == 20) { BAR(); }
      if (n == 20 || n == 21) {
#pragma unroll
        for (int q = 0; q < 4; ++q) gp[q] = *(const LAS f32x4*)(rp0 + (4 * (n - 20) + q) * G_SZ);
        SB();
      }
      if (n == 21 || n == 22) {
#pragma unroll
        for (int e = 0; e < 4; ++e) { const float t = fadd1(fadd1(gq[0][e], gq[1][e]), fadd1(gq[2][e], gq[3][e])); gs4[e] = n == 21 ? t : fadd1(gs4[e], t); }
        asm volatile("" : "+v"(gs4[0]), "+v"(gs4[1]), "+v"(gs4[2]), "+v"(gs4[3])); SB();
      }
      if (n == 20 || n == 21) {
#pragma unroll
        for (int q = 0; q < 4; ++q) gq[q] = gp[q];
      }
      if (n == 23) {
        const unsigned oa = pk(ffma1(gs4[0], r20, bflo(b2r[0])), ffma1(gs4[1], r20, bfhi(b2r[0]))), ob = pk(ffma1(gs4[2], r20, bflo(b2r[1])), ffma1(gs4[3], r20, bfhi(b2r[1])));
        gs4[0] = bflo(oa); gs4[1] = bfhi(oa); gs4[2] = bflo(ob); gs4[3] = bfhi(ob);
        asm volatile("" : "+v"(gs4[0]), "+v"(gs4[1]), "+v"(gs4[2]), "+v"(gs4[3])); SB();
      }
      if (n == 24) {
#pragma unroll
        for (int e = 0; e < 4; ++e) ge[e] = __builtin_amdgcn_exp2f(fmul1(gs4[e], nl2e));
        asm volatile("" : "+v"(ge[0]), "+v"(ge[1]), "+v"(ge[2]), "+v"(ge[3])); SB();
      }
      if (n == 25) {
#pragma unroll
        for (int e = 0; e < 4; ++e) ge[e] = __builtin_amdgcn_rcpf(1.f + ge[e]);
        asm volatile("" : "+v"(ge[0]), "+v"(ge[1]), "+v"(ge[2]), "+v"(ge[3])); SB();
      }
      if (n == 26) {
        g0[0] = pk(fmul1(gs4[0], ge[0]), fmul1(gs4[1], ge[1])); g0[1] = pk(fmul1(gs4[2], ge[2]), fmul1(gs4[3], ge[3]));
        asm volatile("" : "+v"(g0[0]), "+v"(g0[1])); SB();
      }
      if (n == YPOLL) asm volatile("buffer_load_dwordx2 %0, %1, %2, 0 offen sc1" : "=v"(yf) : "v"(yf_off), "s"(sWS) : "memory");
      if (n == YPOLL + YLAG) {
        asm volatile("s_waitcnt vmcnt(0)" : "+v"(yf) :: "memory");
        asm volatile("buffer_load_dwordx4 %0, %1, %2, %3 offen sc1" : "=v"(yb) : "v"(yvo), "s"(sWS), "s"(yso) : "memory");
      }
    }
#pragma unroll
    for (int g4 = 0; g4 < 4; ++g4) { wrp(pb1, ca[1], g4, 0); wrp(pb1, cb[1], g4, 1); }
  }
  asm volatile("s_waitcnt vmcnt(0)" : "+v"(yb) :: "memory");
  if (!__all((((u64)yf[1] << 32) | yf[0]) == tag)) {
    poll_flags(ws, yf_off, tag);
    asm volatile("buffer_load_dwordx4 %0, %1, %2, %3 offen sc1\n\ts_waitcnt vmcnt(0)" : "=&v"(yb) : "v"(yvo), "s"(sWS), "s"(yso) : "memory");
  }
  {
    const unsigned ya = L_Y + (8 * w + (lane >> 3)) * 132 + (lane & 7) * 16;
#pragma unroll
    for (int e = 0; e < 4; ++e) *(LAS unsigned*)(ya + 4 * e) = yb[e];
  }
  BAR();
  {
    unsigned yv[8];
#pragma unroll
    for (int e = 0; e < 8; ++e) yv[e] = *(const LAS u16*)(yra + (e & 3) * 132 + (e >> 2) * 64);
    gate(rp1, r21, g1);
    float t[8]; unsigned pp[8];
#pragma unroll
    for (int e = 1; e < 8; e += 2) yv[e] <<= 16;
    dot2x8(t, yv, xn0[0], xn0[1], xn1[0], xn1[1]);
#pragma unroll
    for (int e = 0; e < 8; e += 2) { pp[e] = pk(t[e], 0.f); pp[e + 1] = pk(0.f, t[e + 1]); }
    dot2x8(t, pp, g0[0], g0[1], g1[0], g1[1]);
#pragma unroll
    for (int e = 0; e < 8; ++e) pp[e] = pk(t[e], xf[e]);
    dot2x8(t, pp, ONES, ONES, ONES, ONES);
    const auto rO = __builtin_amdgcn_make_buffer_rsrc(gout, 0, S_ * D_ * 2, 0x00020000);
    __builtin_amdgcn_raw_buffer_store_b64(u32x2{pk(t[0], t[1]), pk(t[2], t[3])}, rO, xo, 0, 16);
    __builtin_amdgcn_raw_buffer_store_b64(u32x2{pk(t[4], t[5]), pk(t[6], t[7])}, rO, xo, 32 * 896, 16);
  }
}

extern "C" void kernel_launch(void* const* d_in, const int* in_sizes, int n_in, void* d_out, int out_size, void* d_ws, size_t ws_size,
                              hipStream_t stream) {
  (void)in_sizes; (void)n_in; (void)out_size; (void)ws_size;
  (void)hipFuncSetAttribute((const void*)fused_h3_mlp, hipFuncAttributeMaxDynamicSharedMemorySize, LDS_BYTES);
  fused_h3_mlp<<<dim3(8, 8, 4), 512, LDS_BYTES, stream>>>((const u16*)d_in[0], (const u16*)d_in[1], (const u16*)d_in[2], (const u16*)d_in[3],
                                                         (const u16*)d_in[4], (const u16*)d_in[5], (const u16*)d_in[7], (const u16*)d_in[8],
                                                         (const u16*)d_in[9], (const u16*)d_in[10], (u16*)d_out, (char*)d_ws);
}
```
